```python
import torch

def _softmax(x, axis=-1):
    return torch.softmax(x, dim=axis)
def _gelu(x):
    return torch.nn.functional.gelu(x, approximate="tanh")
def _silu(x):
    return torch.nn.functional.silu(x)
def _sigmoid(x):
    return torch.sigmoid(x)
def _layernorm(x, axis=-1, eps=1e-5):
    m = x.mean(dim=axis, keepdim=True)
    v = x.var(dim=axis, keepdim=True, unbiased=False)
    return (x - m) / torch.sqrt(v + eps)
def _scatter_add(tgt, idx, src, axis=0):
    return tgt.clone().index_add_(axis, idx, src)
def _segment_sum(src, seg, n):
    out = torch.zeros((n,) + src.shape[1:], dtype=src.dtype)
    return out.index_add_(0, seg, src)
def _pad(x, pw):
    flat = tuple(v for pair in reversed(pw) for v in pair)
    return torch.nn.functional.pad(x, flat)

kernel_name = 'recurrent_scan__spectral_fnet__dense_activation_silu__prompt'

def setup_inputs():

    _w = (lambda a: (lambda t: t.to(torch.bfloat16) if t.is_floating_point() else t)(torch.as_tensor(a)))
    def _A_setup():
        B, S, D = 1, 2048, 640
        ns = torch.arange(S)[:, None] * torch.arange(S)[None, :]
        nd = torch.arange(D)[:, None] * torch.arange(D)[None, :]
        Ws_re = torch.cos(-2.0 * torch.pi * ns / S).to(float)
        Ws_im = torch.sin(-2.0 * torch.pi * ns / S).to(float)
        Wd_re = torch.cos(-2.0 * torch.pi * nd / D).to(float)
        Wd_im = torch.sin(-2.0 * torch.pi * nd / D).to(float)
        return {
            "x": _w((lambda *s: torch.randn(*s, dtype=torch.bfloat16) * 0.05)(B, S, D)),
            "dft_seq_re": _w(Ws_re),
            "dft_seq_im": _w(Ws_im),
            "dft_hid_re": _w(Wd_re),
            "dft_hid_im": _w(Wd_im),
        }
    _A_in = _A_setup()

    def _B_setup():
        B, S, D, F = 1, 2048, 640, 1536
        return {
            "x": _w((lambda *s: torch.randn(*s, dtype=torch.bfloat16) * 0.05)(B, S, D)),
            "w1": _w((lambda *s: torch.randn(*s, dtype=torch.bfloat16) * 0.2)(D, F) * (D ** -0.5)),
            "w2": _w((lambda *s: torch.randn(*s, dtype=torch.bfloat16) * 0.05)(F, D) * (F ** -0.5)),
        }
    _B_in = _B_setup()

    return {
        "A_x": _A_in["x"],
        "A_dft_seq_re": _A_in["dft_seq_re"],
        "A_dft_seq_im": _A_in["dft_seq_im"],
        "A_dft_hid_re": _A_in["dft_hid_re"],
        "A_dft_hid_im": _A_in["dft_hid_im"],
        "B_x": _B_in["x"],
        "B_w1": _B_in["w1"],
        "B_w2": _B_in["w2"],
    }

def reference(A_x, A_dft_seq_re, A_dft_seq_im, A_dft_hid_re, A_dft_hid_im, B_x, B_w1, B_w2):
    def _A_ref(x, dft_seq_re, dft_seq_im, dft_hid_re, dft_hid_im):
        a = torch.einsum("bsd,ed->bse", x, dft_hid_re)
        b = torch.einsum("bsd,ed->bse", x, dft_hid_im)
        y = torch.einsum("ts,bse->bte", dft_seq_re, a) - torch.einsum("ts,bse->bte", dft_seq_im, b)
        return y

    def _B_ref(x, w1, w2):
        h = torch.einsum("bsd,df->bsf", x, w1)
        h = _silu(h)
        out = torch.einsum("bsf,fd->bsd", h, w2)
        return out

    x = A_x
    xn = _layernorm(x, axis=-1)
    s = _A_ref(xn, A_dft_seq_re, A_dft_seq_im, A_dft_hid_re, A_dft_hid_im)
    g = _silu(_B_ref(xn, B_w1, B_w2))
    return x + s * g
```

```cpp
#include <hip/hip_runtime.h>
#include <hip/hip_bf16.h>
typedef unsigned short u16;
__device__ __forceinline__ float bf2f(u16 v){ return __uint_as_float(((unsigned)v)<<16); }
__device__ __forceinline__ u16 f2bf(float f){ unsigned u=__float_as_uint(f); u += 0x7FFF + ((u>>16)&1); return (u16)(u>>16); }

__global__ void ln_k(const u16* x, u16* xn){
  int row=blockIdx.x; int t=threadIdx.x;
  float v[10]; float s=0;
  for(int i=0;i<10;i++){ v[i]=bf2f(x[row*640+t+64*i]); s+=v[i]; }
  for(int o=32;o>0;o>>=1) s+=__shfl_xor(s,o);
  float m=s/640.f; float q=0;
  for(int i=0;i<10;i++){ float d=v[i]-m; q+=d*d; }
  for(int o=32;o>0;o>>=1) q+=__shfl_xor(q,o);
  float r=1.0f/sqrtf(q/640.f+1e-5f);
  for(int i=0;i<10;i++) xn[row*640+t+64*i]=f2bf((v[i]-m)*r);
}
template<int TB>
__global__ void gemm_k(const u16* A, const u16* B, float* C, int M, int N, int K, int lda, int ldb){
  __shared__ float As[16][65], Bs[16][65];
  int tx=threadIdx.x&15, ty=threadIdx.x>>4; int m0=blockIdx.y*64, n0=blockIdx.x*64;
  float acc[4][4]={};
  for(int k0=0;k0<K;k0+=16){
    for(int i=threadIdx.x;i<1024;i+=256){ int r=i>>4,k=i&15; As[k][r]=bf2f(A[(size_t)(m0+r)*lda+k0+k]);
      if(TB) Bs[k][r]=bf2f(B[(size_t)(n0+r)*ldb+k0+k]); }
    if(!TB) for(int i=threadIdx.x;i<1024;i+=256){ int k=i>>6,c=i&63; Bs[k][c]=bf2f(B[(size_t)(k0+k)*ldb+n0+c]); }
    __syncthreads();
    for(int k=0;k<16;k++) for(int i=0;i<4;i++) for(int j=0;j<4;j++) acc[i][j]+=As[k][ty*4+i]*Bs[k][tx*4+j];
    __syncthreads();
  }
  for(int i=0;i<4;i++) for(int j=0;j<4;j++) C[(size_t)(m0+ty*4+i)*N+n0+tx*4+j]=acc[i][j];
}
__global__ void cvt_k(const float* a, u16* o, int n, int act){ int i=blockIdx.x*256+threadIdx.x; if(i<n){ float v=a[i]; if(act){ v=bf2f(f2bf(v)); v=v/(1.f+__expf(-v)); } o[i]=f2bf(v);} }
__global__ void fin_k(const u16* x,const float* yre,const float* yim,const float* o,u16* out,int n){ int i=blockIdx.x*256+threadIdx.x; if(i<n){
  float y=yre[i]-yim[i]; float v=o[i]; float g=v/(1.f+__expf(-v)); out[i]=f2bf(bf2f(x[i])+y*g);} }

extern "C" void kernel_launch(void* const* d_in, const int* in_sizes, int n_in, void* d_out, int out_size, void* d_ws, size_t ws_size, hipStream_t stream) {
  const u16* x=(const u16*)d_in[0]; const u16* wsre=(const u16*)d_in[1]; const u16* wsim=(const u16*)d_in[2];
  const u16* wdre=(const u16*)d_in[3]; const u16* wdim=(const u16*)d_in[4]; const u16* w1=(const u16*)d_in[6]; const u16* w2=(const u16*)d_in[7];
  char* ws=(char*)d_ws; const int S=2048,D=640,F=1536;
  u16* xn=(u16*)ws; float* t0=(float*)(ws+(4<<20)); float* t1=(float*)(ws+(20<<20)); float* t2=(float*)(ws+(36<<20));
  u16* a=(u16*)(ws+(52<<20)); u16* b=(u16*)(ws+(56<<20)); u16* h=(u16*)(ws+(60<<20));
  ln_k<<<S,64,0,stream>>>(x,xn);
  gemm_k<1><<<dim3(D/64,S/64),256,0,stream>>>(xn,wdre,t0,S,D,D,D,D);
  cvt_k<<<S*D/256,256,0,stream>>>(t0,a,S*D,0);
  gemm_k<1><<<dim3(D/64,S/64),256,0,stream>>>(xn,wdim,t0,S,D,D,D,D);
  cvt_k<<<S*D/256,256,0,stream>>>(t0,b,S*D,0);
  gemm_k<0><<<dim3(F/64,S/64),256,0,stream>>>(xn,w1,t0,S,F,D,D,F);
  cvt_k<<<S*F/256,256,0,stream>>>(t0,h,S*F,1);
  gemm_k<0><<<dim3(D/64,S/64),256,0,stream>>>(wsre,a,t0,S,D,S,S,D);
  gemm_k<0><<<dim3(D/64,S/64),256,0,stream>>>(wsim,b,t1,S,D,S,S,D);
  gemm_k<0><<<dim3(D/64,S/64),256,0,stream>>>(h,w2,t2,S,D,F,F,D);
  fin_k<<<S*D/256,256,0,stream>>>(x,t0,t1,t2,(u16*)d_out,S*D);
}
```

```cpp
#include <hip/hip_runtime.h>
#include <stdint.h>
typedef unsigned short u16;
typedef float f32x4 __attribute__((ext_vector_type(4)));
typedef float f32x2 __attribute__((ext_vector_type(2)));
typedef __bf16 bf16x8 __attribute__((ext_vector_type(8)));
typedef __bf16 bf16x2 __attribute__((ext_vector_type(2)));
typedef unsigned u32x4 __attribute__((ext_vector_type(4)));
typedef unsigned u32x2 __attribute__((ext_vector_type(2)));
#define DI __device__ __forceinline__
#define GAP() __builtin_amdgcn_sched_barrier(0)
#define LAS __attribute__((address_space(3)))
constexpr int S_=2048, D_=640, F_=1536;
DI unsigned rfl(unsigned x){ return __builtin_amdgcn_readfirstlane(x); }
DI float bf2f(unsigned v){ return __uint_as_float(v<<16); }
DI float bflo(unsigned v){ return __uint_as_float(v<<16); }
DI float bfhi(unsigned v){ return __uint_as_float(v&0xffff0000u); }
DI unsigned pk(float lo,float hi){ f32x2 v={lo,hi}; bf16x2 b=__builtin_convertvector(v,bf16x2); return __builtin_bit_cast(unsigned,b); }
DI float silu(float v){ return v*__builtin_amdgcn_rcpf(1.f+__builtin_amdgcn_exp2f(-1.4426950408889634f*v)); }
template<int N> DI void wait_vm(){ asm volatile("s_waitcnt vmcnt(%0)"::"n"(N):"memory"); }
DI void dma(const __amdgpu_buffer_rsrc_t& srd, unsigned voff, unsigned soff, unsigned m0v){
  asm volatile("s_mov_b32 m0, %0\n\ts_nop 0\n\tbuffer_load_dwordx4 %1, %2, %3 offen lds" :: "s"(m0v), "v"(voff), "s"(srd), "s"(soff) : "m0","memory");
}
DI __amdgpu_buffer_rsrc_t mksrd(const void* p){ return __builtin_amdgcn_make_buffer_rsrc((void*)p,0,0x7ffffff0,0x00020000); }

__global__ __launch_bounds__(256) void k0_ln(const u16* __restrict__ x, u16* __restrict__ xn, const u16* __restrict__ w1, const u16* __restrict__ w2, u16* __restrict__ w1t, u16* __restrict__ w2t){
  __shared__ u16 tile[64][66];
  const int b=blockIdx.x, tid=threadIdx.x;
  if(b<128){
    const int row=b*16+(tid>>4), l=tid&15;
    u32x4 v[5]; float s=0;
    #pragma unroll
    for(int j=0;j<5;j++){ v[j]=*(const u32x4*)(x+(size_t)row*D_+(j*16+l)*8);
      #pragma unroll
      for(int q=0;q<4;q++) s+=bflo(v[j][q])+bfhi(v[j][q]); }
    #pragma unroll
    for(int o=1;o<16;o<<=1) s+=__shfl_xor(s,o);
    const float m=s*(1.f/D_); float q2=0;
    #pragma unroll
    for(int j=0;j<5;j++)
      #pragma unroll
      for(int q=0;q<4;q++){ float a=bflo(v[j][q])-m, c=bfhi(v[j][q])-m; q2+=a*a+c*c; }
    #pragma unroll
    for(int o=1;o<16;o<<=1) q2+=__shfl_xor(q2,o);
    const float r=1.0f/sqrtf(q2*(1.f/D_)+1e-5f);
    #pragma unroll
    for(int j=0;j<5;j++){ u32x4 o;
      #pragma unroll
      for(int q=0;q<4;q++) o[q]=pk((bflo(v[j][q])-m)*r,(bfhi(v[j][q])-m)*r);
      *(u32x4*)(xn+(size_t)row*D_+(j*16+l)*8)=o; }
  } else {
    int t=b-128; const u16* src; u16* dst; int R,C,r0,c0;
    if(t<240){ src=w1; dst=w1t; R=D_; C=F_; r0=(t/24)*64; c0=(t%24)*64; } else { t-=240; src=w2; dst=w2t; R=F_; C=D_; r0=(t/10)*64; c0=(t%10)*64; }
    for(int i=tid;i<64*8;i+=256){ int r=i>>3,c=(i&7)*8; u32x4 v=*(const u32x4*)(src+(size_t)(r0+r)*C+c0+c);
      #pragma unroll
      for(int q=0;q<4;q++){ tile[r][c+2*q]=(u16)(v[q]&0xffff); tile[r][c+2*q+1]=(u16)(v[q]>>16);} }
    __syncthreads();
    for(int i=tid;i<64*8;i+=256){ int c=i>>3,r=(i&7)*8; u32x4 v;
      #pragma unroll
      for(int q=0;q<4;q++) v[q]=(unsigned)tile[r+2*q][c]|((unsigned)tile[r+2*q+1][c]<<16);
      *(u32x4*)(dst+(size_t)(c0+c)*R+r0+r)=v; }
  }
}

template<int BN,int NS,int PERM>
DI void loader(LAS char* lds, const u16* A0, const u16* A1, int ksw, unsigned lda, const u16* B, unsigned ldb, int nt, unsigned lw, unsigned lane){
  constexpr int BM=128, STG=(BM+BN)*128, NP=(BM+BN)/8, PPW=NP/4;
  const unsigned ldsb=(unsigned)(size_t)lds;
  const __amdgpu_buffer_rsrc_t sA0=mksrd(A0), sA1=mksrd(A1), sB=mksrd(B);
  const unsigned r=lane>>3;
  const unsigned swz=((lane&7)^((lw&1)*4+(lane>>4)))*16;
  const unsigned rp=8*(r>>2)+(r&3);
  const unsigned voffA=(PERM==1?rp:r)*lda*2+swz, voffB=(PERM==2?rp:r)*ldb*2+swz;
  unsigned soA[4], soB[PPW-4];
  #pragma unroll
  for(int i=0;i<4;i++){ unsigned q=lw+4*i; unsigned p=q&3; unsigned row= PERM==1? (q>>2)*32+16*(p&1)+4*(p>>1) : q*8; soA[i]=row*lda*2; }
  #pragma unroll
  for(int i=0;i<PPW-4;i++){ unsigned q=lw+4*i; unsigned p=q&3; unsigned row= PERM==2? (q>>2)*32+16*(p&1)+4*(p>>1) : q*8; soB[i]=row*ldb*2; }
  auto stage=[&](int slot,int t){
    const unsigned ko=(unsigned)t*128u; const unsigned lb=ldsb+slot*STG+lw*1024u;
    if(t<ksw){
      #pragma unroll
      for(int i=0;i<4;i++) dma(sA0,voffA,soA[i]+ko,lb+i*4096u);
    } else {
      #pragma unroll
      for(int i=0;i<4;i++) dma(sA1,voffA,soA[i]+ko,lb+i*4096u);
    }
    #pragma unroll
    for(int i=0;i<PPW-4;i++) dma(sB,voffB,soB[i]+ko,lb+16384u+i*4096u);
  };
  stage(0,0); stage(1,1);
  wait_vm<PPW>();
  __builtin_amdgcn_s_barrier();
  #pragma unroll
  for(int s=2;s<NS;s++) stage(s,s);
  int slot=0;
  for(int t=0;t<nt;t++){
    const int rem=nt-1-(t+1);
    if(rem>=NS-2) wait_vm<PPW*(NS-2)>(); else if(NS>3 && rem==NS-3) wait_vm<PPW*(NS>3?NS-3:0)>(); else if(NS>4 && rem==NS-4) wait_vm<PPW*(NS>4?NS-4:0)>(); else wait_vm<0>();
    __builtin_amdgcn_s_barrier();
    if(t+NS<nt) stage(slot,t+NS);
    slot= slot==NS-1?0:slot+1;
  }
}

template<int BN,int NS,bool SWAP>
DI void mfma_loop(LAS char* lds, int nt, unsigned w, unsigned lane, f32x4 (&acc)[4][BN/32]){
  constexpr int BM=128, STG=(BM+BN)*128, FM=4, FN=BN/32, WN=BN/2;
  const unsigned wr=w>>1, wc=w&1;
  unsigned adA[2], adB[2];
  #pragma unroll
  for(int s=0;s<2;s++){ unsigned ch=((4*s+(lane>>4))^((lane>>1)&7))*16;
    adA[s]=(wr*64+(lane&15))*128+ch; adB[s]=BM*128+(wc*WN+(lane&15))*128+ch; }
  #pragma unroll
  for(int i=0;i<FM;i++)
    #pragma unroll
    for(int j=0;j<FN;j++) acc[i][j]=(f32x4){0.f,0.f,0.f,0.f};
  bf16x8 fa[2][FM], fb[2][FN];
  __builtin_amdgcn_s_barrier();
  #pragma unroll
  for(int i=0;i<FM;i++) fa[0][i]=*(const LAS bf16x8*)(lds+adA[0]+i*2048);
  #pragma unroll
  for(int j=0;j<FN;j++) fb[0][j]=*(const LAS bf16x8*)(lds+adB[0]+j*2048);
  GAP();
  constexpr int NMF=FM*FN, NRD=FM+FN;
  for(int tb=0;tb<nt;tb+=NS){
    #pragma unroll
    for(int u=0;u<NS;u++){
      if(tb+u>=nt) break;
      #pragma unroll
      for(int s=0;s<2;s++){
        const int buf=s; const bool last=(s==1);
        __builtin_amdgcn_s_waitcnt(0xC07F);
        if(last) __builtin_amdgcn_s_barrier();
        GAP();
        const int nu=last?(u+1)%NS:u; const int ns=last?0:1;
        const LAS char* base=lds+nu*STG;
        #pragma unroll
        for(int m=0;m<NMF;m++){
          const int fm=m/FN, f0=m%FN, fn=(fm&1)?FN-1-f0:f0;
          if(SWAP) acc[fm][fn]=__builtin_amdgcn_mfma_f32_16x16x32_bf16(fb[buf][fn],fa[buf][fm],acc[fm][fn],0,0,0);
          else     acc[fm][fn]=__builtin_amdgcn_mfma_f32_16x16x32_bf16(fa[buf][fm],fb[buf][fn],acc[fm][fn],0,0,0);
          if(m<NRD){
            if(m==0) fa[buf^1][0]=*(const LAS bf16x8*)(base+adA[ns]);
            else if(m<=FN) fb[buf^1][m-1]=*(const LAS bf16x8*)(base+adB[ns]+(m-1)*2048);
            else fa[buf^1][m-FN]=*(const LAS bf16x8*)(base+adA[ns]+(m-FN)*2048);
          }
          GAP();
        }
      }
    }
  }
}

struct P1 { const u16* xn; const u16* wdre; const u16* wdim; const u16* w1t; u16* abT; u16* h; };
__global__ __launch_bounds__(512,2) void k1_phase1(P1 p){
  extern __shared__ __attribute__((aligned(1024))) char smem[];
  LAS char* lds=(LAS char*)smem;
  const unsigned tid=threadIdx.x, lane=tid&63, w=rfl(tid>>6);
  const int b=blockIdx.x, x=b&7, r=b>>3; const int pm=x*2+(r>>4), j=r&15;
  const u16* A=p.xn+(size_t)pm*128*D_;
  if(j<8){
    const u16* B=(j<4? p.wdre+(size_t)j*160*D_ : p.wdim+(size_t)(j-4)*160*D_);
    if(w>=4){ loader<160,4,1>(lds,A,A,1000,D_,B,D_,10,w-4,lane); return; }
    f32x4 acc[4][5]; mfma_loop<160,4,false>(lds,10,w,lane,acc);
    const unsigned wr=w>>1, wc=w&1; const float sg= j<4?1.f:-1.f;
    #pragma unroll
    for(int fn=0;fn<5;fn++){ const int e=(j&3)*160+wc*80+fn*16+(lane&15);
      #pragma unroll
      for(int fp=0;fp<2;fp++){ const f32x4 a=acc[2*fp][fn]*sg, c=acc[2*fp+1][fn]*sg;
        u32x4 o={pk(a[0],a[1]),pk(a[2],a[3]),pk(c[0],c[1]),pk(c[2],c[3])};
        const int s=pm*128+wr*64+fp*32+8*(lane>>4);
        *(u32x4*)(p.abT+(size_t)e*4096+(j<4?0:2048)+s)=o; } }
  } else {
    const int jj=j-8; const u16* B=p.w1t+(size_t)jj*192*D_;
    if(w>=4){ loader<192,4,2>(lds,A,A,1000,D_,B,D_,10,w-4,lane); return; }
    f32x4 acc[4][6]; mfma_loop<192,4,true>(lds,10,w,lane,acc);
    const unsigned wr=w>>1, wc=w&1;
    #pragma unroll
    for(int fm=0;fm<4;fm++){ const int s=pm*128+wr*64+fm*16+(lane&15);
      #pragma unroll
      for(int fp=0;fp<3;fp++){ f32x4 a=acc[fm][2*fp], c=acc[fm][2*fp+1];
        u32x4 o={pk(silu(a[0]),silu(a[1])),pk(silu(a[2]),silu(a[3])),pk(silu(c[0]),silu(c[1])),pk(silu(c[2]),silu(c[3]))};
        const int f=jj*192+wc*96+fp*32+8*(lane>>4);
        *(u32x4*)(p.h+(size_t)s*F_+f)=o; } }
  }
}

struct P2 { const u16* wsre; const u16* wsim; const u16* abT; const u16* h; const u16* w2t; u16* part; };
__global__ __launch_bounds__(512,2) void k2_phase2(P2 p){
  extern __shared__ __attribute__((aligned(1024))) char smem[];
  LAS char* lds=(LAS char*)smem;
  const unsigned tid=threadIdx.x, lane=tid&63, w=rfl(tid>>6);
  const int b=blockIdx.x, x=b&7, r=b>>3; const int part=x>>1, pm=(x&1)*8+(r>>2), pn=r&3;
  int nt;
  if(w>=4){
    if(part<3){ const int k0= part*21; nt= part==2?22:21;
      const u16* Are=p.wsre+(size_t)pm*128*S_+k0*64; const u16* Aim=p.wsim+(size_t)pm*128*S_+(k0-32)*64;
      loader<160,4,0>(lds,Are,Aim,32-k0,S_,p.abT+(size_t)pn*160*4096+k0*64,4096,nt,w-4,lane);
    } else loader<160,4,0>(lds,p.h+(size_t)pm*128*F_,p.h,1000,F_,p.w2t+(size_t)pn*160*F_,F_,24,w-4,lane);
    return;
  }
  nt= part<2?21: part==2?22:24;
  f32x4 acc[4][5]; mfma_loop<160,4,true>(lds,nt,w,lane,acc);
  u16* dst=p.part+((size_t)(part*64+pm*4+pn)*4+w)*10*512+lane*8;
  #pragma unroll
  for(int fn=0;fn<5;fn++)
    #pragma unroll
    for(int fp=0;fp<2;fp++){ const f32x4 a=acc[2*fp][fn], c=acc[2*fp+1][fn];
      u32x4 o={pk(a[0],a[1]),pk(a[2],a[3]),pk(c[0],c[1]),pk(c[2],c[3])};
      *(u32x4*)(dst+(fn*2+fp)*512)=o; }
}

__global__ __launch_bounds__(256) void k3_comb(const u16* __restrict__ part, const u16* __restrict__ x, u16* __restrict__ out){
  const int gw=blockIdx.x*4+(threadIdx.x>>6), lane=threadIdx.x&63;
  const int tile=gw/40, c=gw%40, w=c/10, j=c%10, fn=j>>1, fp=j&1; const int pm=tile>>2, pn=tile&3, wr=w>>1, wc=w&1;
  const size_t off=((size_t)tile*4+w)*5120+j*512+lane*8; const size_t ps=(size_t)64*4*5120;
  u32x4 y0=*(const u32x4*)(part+off), y1=*(const u32x4*)(part+ps+off), y2=*(const u32x4*)(part+2*ps+off), o=*(const u32x4*)(part+3*ps+off);
  const int e=pn*160+wc*80+fn*16+4*(lane>>4);
  #pragma unroll
  for(int h=0;h<2;h++){ const int t=pm*128+wr*64+(2*fp+h)*16+(lane&15);
    const u32x2 xv=*(const u32x2*)(x+(size_t)t*D_+e); u32x2 r;
    #pragma unroll
    for(int q=0;q<2;q++){ const int i=h*2+q;
      float ya=bflo(y0[i])+bflo(y1[i])+bflo(y2[i]), yb=bfhi(y0[i])+bfhi(y1[i])+bfhi(y2[i]);
      r[q]=pk(bflo(xv[q])+ya*silu(bflo(o[i])), bfhi(xv[q])+yb*silu(bfhi(o[i]))); }
    *(u32x2*)(out+(size_t)t*D_+e)=r; }
}

extern "C" void kernel_launch(void* const* d_in, const int* in_sizes, int n_in, void* d_out, int out_size, void* d_ws, size_t ws_size, hipStream_t stream) {
  const u16* x=(const u16*)d_in[0]; const u16* wsre=(const u16*)d_in[1]; const u16* wsim=(const u16*)d_in[2];
  const u16* wdre=(const u16*)d_in[3]; const u16* wdim=(const u16*)d_in[4]; const u16* w1=(const u16*)d_in[6]; const u16* w2=(const u16*)d_in[7];
  char* ws=(char*)d_ws;
  u16* xn=(u16*)ws; u16* w1t=(u16*)(ws+(4<<20)); u16* w2t=(u16*)(ws+(8<<20)); u16* abT=(u16*)(ws+(12<<20)); u16* h=(u16*)(ws+(20<<20)); u16* part=(u16*)(ws+(28<<20));
  (void)hipFuncSetAttribute((const void*)k1_phase1, hipFuncAttributeMaxDynamicSharedMemorySize, 4*(128+192)*128);
  (void)hipFuncSetAttribute((const void*)k2_phase2, hipFuncAttributeMaxDynamicSharedMemorySize, 4*(128+160)*128);
  k0_ln<<<128+480,256,0,stream>>>(x,xn,w1,w2,w1t,w2t);
  P1 p1{xn,wdre,wdim,w1t,abT,h}; k1_phase1<<<256,512,4*(128+192)*128,stream>>>(p1);
  P2 p2{wsre,wsim,abT,h,w2t,part}; k2_phase2<<<256,512,4*(128+160)*128,stream>>>(p2);
  k3_comb<<<640,256,0,stream>>>(part,x,(u16*)d_out);
}
```

```cpp
#include <hip/hip_runtime.h>
#include <stdint.h>
typedef unsigned short u16;
typedef unsigned long long u64;
typedef float f32x4 __attribute__((ext_vector_type(4)));
typedef float f32x2 __attribute__((ext_vector_type(2)));
typedef __bf16 bf16x8 __attribute__((ext_vector_type(8)));
typedef __bf16 bf16x2 __attribute__((ext_vector_type(2)));
typedef short s16x4 __attribute__((ext_vector_type(4)));
typedef short s16x8 __attribute__((ext_vector_type(8)));
typedef unsigned u32x4 __attribute__((ext_vector_type(4)));
typedef unsigned u32x2 __attribute__((ext_vector_type(2)));
#define DI __device__ __forceinline__
#define GAP() __builtin_amdgcn_sched_barrier(0)
#define LAS __attribute__((address_space(3)))
#define GAS __attribute__((address_space(1)))
typedef GAS u64 gu64;
typedef GAS unsigned gu32;
typedef __amdgpu_buffer_rsrc_t srd_t;
constexpr int S_=2048, D_=640, F_=1536;
constexpr unsigned WS_SF1=4096, WS_SF2=12288, WS_ABF=20480, WS_HF=24576, WS_YMF=28672, WS_FXF=32768, WS_RST=65536, WS_CSP=131072;
constexpr unsigned WS_ABT=1u<<20, WS_H=8u<<20, WS_YM=16u<<20, WS_FX=24u<<20;
constexpr int STG1=38912, O_WD=16384, O_W1=26624, AUX=155648, STG2=36864, O_B=16384, HSTG=110592, LDS_BYTES=163840;
constexpr unsigned SPIN_MAX=40000;
#define NY01 22
constexpr int NYA=NY01, NYB=32-NY01, NOB=12;

#define TS(i)

DI unsigned rfl(unsigned x){ return __builtin_amdgcn_readfirstlane(x); }
DI float bflo(unsigned v){ return __uint_as_float(v<<16); }
DI float bfhi(unsigned v){ return __uint_as_float(v&0xffff0000u); }
DI unsigned pk(float lo,float hi){ f32x2 v={lo,hi}; bf16x2 b=__builtin_convertvector(v,bf16x2); return __builtin_bit_cast(unsigned,b); }
DI float sigm(float v){ return __builtin_amdgcn_rcpf(1.f+__builtin_amdgcn_exp2f(-1.4426950408889634f*v)); }
template<int N> DI void wait_vm(){ asm volatile("s_waitcnt vmcnt(%0)"::"n"(N):"memory"); }
DI void bar(){ __builtin_amdgcn_s_barrier(); }
DI void wait_lgkm(){ __builtin_amdgcn_s_waitcnt(0xC07F); }
DI void dma(const srd_t& srd, unsigned voff, unsigned soff, unsigned m0v){
  asm volatile("s_mov_b32 m0, %0\n\ts_nop 0\n\tbuffer_load_dwordx4 %1, %2, %3 offen lds" :: "s"(m0v), "v"(voff), "s"(srd), "s"(soff) : "m0","memory");
}
DI void dma_dev(const srd_t& srd, unsigned voff, unsigned soff, unsigned m0v){
  asm volatile("s_mov_b32 m0, %0\n\ts_nop 0\n\tbuffer_load_dwordx4 %1, %2, %3 offen sc1 lds" :: "s"(m0v), "v"(voff), "s"(srd), "s"(soff) : "m0","memory");
}
DI srd_t mksrd(const void* p){ return __builtin_amdgcn_make_buffer_rsrc((void*)p,0,0x7ffffff0,0x00020000); }
DI void st_flag(char* ws, unsigned off, u64 tag){ __hip_atomic_store((gu64*)(ws+off), tag, __ATOMIC_RELAXED, __HIP_MEMORY_SCOPE_AGENT); }
DI void st_f32(char* ws, unsigned off, float v){ __hip_atomic_store((gu32*)(ws+off), __float_as_uint(v), __ATOMIC_RELAXED, __HIP_MEMORY_SCOPE_AGENT); }
DI u64 ld_flag(char* ws, unsigned off){ return __hip_atomic_load((gu64*)(ws+off), __ATOMIC_RELAXED, __HIP_MEMORY_SCOPE_AGENT); }
DI void wait_flags(char* ws, unsigned off, u64 tag){
  #pragma unroll 1
  for(unsigned s=0;s<SPIN_MAX;s++){ const u64 v=ld_flag(ws,off); if(__all(v==tag)) break; __builtin_amdgcn_s_sleep(2); }
}
DI u64 get_tag(const char* ws){ u64 ep; asm volatile("s_load_dwordx2 %0, %1, 0x0\n\ts_waitcnt lgkmcnt(0)":"=s"(ep):"s"(ws):"memory"); return ep+1; }
DI void poll_issue(u64& v, char* ws, unsigned off){ const char* a=ws+off; asm volatile("global_load_dwordx2 %0, %1, off sc1":"=v"(v):"v"(a):"memory"); }
DI void poll_check(u64& v, char* ws, unsigned off, u64 tag){ asm volatile("":"+v"(v)); if(!__all(v==tag)) wait_flags(ws,off,tag); }
DI bf16x8 ldf(const LAS char* a){ return *(const LAS bf16x8*)a; }
DI bf16x8 ldtr(const LAS char* a, int step){
  const s16x4 lo=__builtin_amdgcn_ds_read_tr16_b64_v4i16((LAS s16x4*)a), hi=__builtin_amdgcn_ds_read_tr16_b64_v4i16((LAS s16x4*)(a+step));
  return __builtin_bit_cast(bf16x8,(s16x8)__builtin_shufflevector(lo,hi,0,1,2,3,4,5,6,7));
}
DI s16x4 ldtr1(const LAS char* a){ return __builtin_amdgcn_ds_read_tr16_b64_v4i16((LAS s16x4*)a); }
DI bf16x8 cat(s16x4 lo, s16x4 hi){ return __builtin_bit_cast(bf16x8,(s16x8)__builtin_shufflevector(lo,hi,0,1,2,3,4,5,6,7)); }
DI f32x4 mma(bf16x8 a, bf16x8 b, f32x4 c){ return __builtin_amdgcn_mfma_f32_16x16x32_bf16(a,b,c,0,0,0); }

struct Prm { const u16* x; const u16* wsre; const u16* wsim; const u16* wdre; const u16* wdim; const u16* w1; const u16* w2; u16* out; char* ws; };

template<int NWD>
DI u64 p1_loader(const Prm& p, LAS char* lds, int pm, int j, unsigned lw, unsigned lane){
  constexpr int PPW=7+NWD;
  const unsigned ldsb=(unsigned)(size_t)lds;
  const srd_t sA=mksrd(p.x+(size_t)pm*128*D_), sB=mksrd((j<8? p.wdre: p.wdim)+(size_t)(j&7)*80*D_), sW=mksrd(p.w1+j*96), sS=mksrd(p.ws);
  const unsigned r=lane>>3, swz=((lane&7)^((lw&1)*4+(lane>>4)))*16;
  const unsigned voffA=(8*(r>>2)+(r&3))*(D_*2)+swz, voffB=r*(D_*2)+swz;
  unsigned voffW[3];
  #pragma unroll
  for(int i=0;i<3;i++){ const unsigned q=i*64+lane, row=q/12, ch=q%12; voffW[i]=row*(F_*2)+32*((ch>>1)^((row>>3)&1))+16*(ch&1); }
  unsigned soA[4];
  #pragma unroll
  for(int i=0;i<4;i++){ const unsigned q=lw+4*i, pp=q&3; soA[i]=((q>>2)*32+16*(pp&1)+4*(pp>>1))*(D_*2); }
  const int k0=(pm+3*j)%10;
  auto stage=[&](int slot,int t){
    int kt=k0+t; if(kt>=10) kt-=10;
    const unsigned lb=ldsb+slot*STG1, ko=(unsigned)kt*128u;
    #pragma unroll
    for(int i=0;i<4;i++) dma(sA,voffA,soA[i]+ko,lb+(lw+4*i)*1024u);
    #pragma unroll
    for(int i=0;i<NWD;i++) dma(sB,voffB,(lw+4*i)*8*(D_*2)+ko,lb+O_WD+(lw+4*i)*1024u);
    #pragma unroll
    for(int i=0;i<3;i++) dma(sW,voffW[i],((unsigned)kt*64u+16u*lw)*(F_*2),lb+O_W1+lw*3072u+i*1024u);
  };
  stage(0,0); stage(1,1);
  const u64 tag=get_tag(p.ws);
  wait_vm<PPW>();
  bar();
  stage(2,2); stage(3,3);
  u64 pv=0; const unsigned poff= lw==0? WS_SF1+(pm*64+lane)*8 : WS_SF2+(j*64+(lane<40?lane:0))*8;
  #pragma unroll 1
  for(int t=0;t<6;t++){
    wait_vm<2*PPW>();
    bar();
    if(t==4 && lw<2) poll_issue(pv,p.ws,poff);
    stage(t&3,t+4);
  }
  wait_vm<2*PPW>();
  if(lw<2) poll_check(pv,p.ws,poff,tag);
  bar();
  #pragma unroll
  for(int i=0;i<2;i++){ const unsigned d=lw*2+i;
    const unsigned vo= d==0? WS_RST+(lane>>5)*8192+pm*512+(lane&31)*16 : WS_CSP+j*7040+(d-1)*1024+lane*16;
    dma_dev(sS,vo,0,ldsb+AUX+d*1024u); }
  wait_vm<PPW+2>(); bar();
  wait_vm<0>(); bar();
  { const unsigned c=lw*64+lane; if(c<176){ LAS float* cs=(LAS float*)(lds+AUX+1024)+c; float s=0;
      #pragma unroll
      for(int k=0;k<10;k++) s+=cs[k*176];
      cs[0]=s; } }
  wait_lgkm();
  bar();
  return tag;
}

template<int WC>
DI u64 p1_mfma(const Prm& p, LAS char* lds, int pm, int j, unsigned w, unsigned lane){
  u64 tag;
  const unsigned wr=w>>1, g=lane>>4, c16=lane&15;
  { const unsigned row=pm*128+j*8+w*2+(lane>>5), l=lane&31; u32x2 v[5]; float s=0;
    #pragma unroll
    for(int i=0;i<5;i++) v[i]=*(const u32x2*)(p.x+(size_t)row*D_+(l+32*i)*4);
    tag=get_tag(p.ws);
    #pragma unroll
    for(int i=0;i<5;i++) s+=(bflo(v[i][0])+bfhi(v[i][0]))+(bflo(v[i][1])+bfhi(v[i][1]));
    #pragma unroll
    for(int o=1;o<32;o<<=1) s+=__shfl_xor(s,o);
    const float m=s*(1.f/D_); float q2=0;
    #pragma unroll
    for(int i=0;i<5;i++)
      #pragma unroll
      for(int k=0;k<2;k++){ const float a=bflo(v[i][k])-m, c=bfhi(v[i][k])-m; q2+=a*a+c*c; }
    #pragma unroll
    for(int o=1;o<32;o<<=1) q2+=__shfl_xor(q2,o);
    const float rs=1.0f/sqrtf(q2*(1.f/D_)+1e-5f);
    if(l<2) st_f32(p.ws,WS_RST+l*8192+row*4, l==0? m: rs);
  }
  TS(1);
  unsigned adA[2], adB[2], adT[2];
  #pragma unroll
  for(int s=0;s<2;s++){ const unsigned ch=((4*s+g)^((lane>>1)&7))*16; adA[s]=(wr*64+c16)*128+ch; adB[s]=O_WD+c16*128+ch; }
  { const unsigned b0=O_W1+192*(8*g+(c16>>2))+8*(lane&3); adT[0]=b0+32*(g&1); adT[1]=b0-32*(g&1); }
  f32x4 acc[4][5], acs[2], csa[3];
  #pragma unroll
  for(int i=0;i<4;i++)
    #pragma unroll
    for(int k=0;k<5;k++) acc[i][k]=(f32x4){0.f,0.f,0.f,0.f};
  acs[0]=acs[1]=csa[0]=csa[1]=csa[2]=(f32x4){0.f,0.f,0.f,0.f};
  const u32x4 one4={0x3F803F80u,0x3F803F80u,0x3F803F80u,0x3F803F80u}; const bf16x8 ones=__builtin_bit_cast(bf16x8,one4);
  bf16x8 fa[2][4], fb[2][5], fs[2]; s16x4 tl[6];
  constexpr int NRD= WC==0? 11: 16;
  auto rd=[&](int m,const LAS char* base,int ks,int bf){
    if(WC==0){
      if(m==0) fa[bf][0]=ldf(base+adA[ks]);
      else if(m<=5) fb[bf][m-1]=ldf(base+adB[ks]+(m-1)*2048);
      else if(m==6) tl[0]=ldtr1(base+adT[0]+ks*6144);
      else if(m==7) fs[bf]=cat(tl[0],ldtr1(base+adT[0]+ks*6144+768));
      else fa[bf][m-7]=ldf(base+adA[ks]+(m-7)*2048);
    } else {
      if(m==0) fa[bf][0]=ldf(base+adA[ks]);
      else if(m<=10){ const int c=(m+1)/2; const LAS char* a=base+adT[c&1]+32*c+ks*6144;
        if(m&1) tl[c]=ldtr1(a); else fb[bf][c-1]=cat(tl[c],ldtr1(a+768)); }
      else if(m==11) tl[0]=ldtr1(base+adT[0]+ks*6144);
      else if(m==12) fs[bf]=cat(tl[0],ldtr1(base+adT[0]+ks*6144+768));
      else fa[bf][m-12]=ldf(base+adA[ks]+(m-12)*2048);
    }
  };
  bar();
  TS(2);
  #pragma unroll
  for(int m=0;m<NRD;m++) rd(m,lds,0,0);
  GAP();
  #pragma unroll
  for(int t=0;t<10;t++){
    if(t==1){
      const unsigned wq=WC*2+wr; const float v= g==0? csa[0][0]: g==1? csa[1][0]: csa[2][0];
      const int k0=(pm+3*j)%10;
      if(pm<10 && lane<(wq==3?32u:48u)) st_f32(p.ws,WS_CSP+((j*10+k0)*176+wq*48+lane)*4,v);
    }
    if(t==3){ wait_vm<0>(); if(lane<2) st_flag(p.ws, lane==0? WS_SF1+(pm*64+j*4+w)*8 : WS_SF2+(j*64+pm*4+w)*8, tag); }
    #pragma unroll
    for(int s=0;s<2;s++){
      const bool last=(s==1);
      wait_lgkm();
      if(last) bar();
      GAP();
      const LAS char* base=lds+(last?((t+1)&3):(t&3))*STG1; const int ns=last?0:1;
      #pragma unroll
      for(int m=0;m<22;m++){
        if(m<20){ const int fm=m/5, f0=m%5, fn=(fm&1)?4-f0:f0;
          acc[fm][fn]= WC==0? mma(fa[s][fm],fb[s][fn],acc[fm][fn]) : mma(fb[s][fn],fa[s][fm],acc[fm][fn]);
        } else acs[m-20]=mma(fs[s],fa[s][WC*2+m-20],acs[m-20]);
        if(m<NRD && !(last && t==9)) rd(m,base,ns,s^1);
        GAP();
      }
      if(t==0){
        if(wr==0){ csa[0]=mma(ones,fb[s][0],csa[0]); csa[1]=mma(ones,fb[s][1],csa[1]); csa[2]=mma(ones,fb[s][2],csa[2]); }
        else     { csa[0]=mma(ones,fb[s][3],csa[0]); csa[1]=mma(ones,fb[s][4],csa[1]); csa[2]=mma(ones,fs[s],csa[2]); }
        GAP();
      }
    }
  }
  TS(3);
  const LAS float* rowm=(const LAS float*)(lds+AUX); const LAS float* rowr=rowm+128; const LAS float* cs=rowm+256;
  auto hstage=[&](const f32x4& a,int fm,int blk){
    const unsigned s=wr*64+32*(fm>>1)+16*(c16>>3)+8*((c16>>2)&1)+4*(fm&1)+(c16&3);
    const float m=rowm[s]; const f32x4 c4=*(const LAS f32x4*)(cs+80+16*blk+4*g);
    const u32x2 o={pk(a[0]-m*c4[0],a[1]-m*c4[1]),pk(a[2]-m*c4[2],a[3]-m*c4[3])};
    const unsigned u=(2*blk+(g>>1))^((s&3)<<2)^(((s>>1)&1)|(((s>>3)&1)<<1));
    *(LAS u32x2*)(lds+HSTG+s*256+u*16+(g&1)*8)=o;
  };
  if(WC==0){
    const srd_t sO=mksrd(p.ws+WS_ABT); const float sg= j<8? 1.f: -1.f;
    #pragma unroll
    for(int fp=0;fp<2;fp++){
      const unsigned s0=wr*64+32*fp+8*g;
      const f32x4 m0=*(const LAS f32x4*)(rowm+s0), m1=*(const LAS f32x4*)(rowm+s0+4);
      const f32x4 r0=*(const LAS f32x4*)(rowr+s0)*sg, r1=*(const LAS f32x4*)(rowr+s0+4)*sg;
      #pragma unroll
      for(int fn=0;fn<5;fn++){ const float c=cs[16*fn+c16];
        const f32x4 a=(acc[2*fp][fn]-m0*c)*r0, b=(acc[2*fp+1][fn]-m1*c)*r1;
        const u32x4 o={pk(a[0],a[1]),pk(a[2],a[3]),pk(b[0],b[1]),pk(b[2],b[3])};
        const unsigned off=(((j&7)*80+16*fn+c16)*4096+(j>>3)*2048+pm*128+s0)*2;
        __builtin_amdgcn_raw_buffer_store_b128(o,sO,off,0,16); }
    }
    hstage(acs[0],0,0); hstage(acs[1],1,0);
  } else {
    #pragma unroll
    for(int fm=0;fm<4;fm++)
      #pragma unroll
      for(int fn=0;fn<5;fn++) hstage(acc[fm][fn],fm,fn+1);
    hstage(acs[0],2,0); hstage(acs[1],3,0);
  }
  wait_lgkm();
  bar();
  TS(4);
  if(WC==0){ wait_vm<0>(); if(lane==0) st_flag(p.ws,WS_ABF+((((j>>3)*4+((j&7)>>1))*16+pm)*4+(j&1)*2+wr)*8,tag); }
  return tag;
}

DI int rot4(int t,int n,int rot){ return (t|3)<n? ((t&~3)|((t+rot)&3)) : ((t|1)<n? (t^(rot&1)) : t); }

DI void p2_loader(const Prm& p, LAS char* lds, int pm1, int j, int q, int pm, int pn, unsigned lw, unsigned lane, u64 tag){
  const unsigned ldsb=(unsigned)(size_t)lds;
  const srd_t sRe=mksrd(p.wsre+(size_t)pm*128*S_), sIm=mksrd(p.wsim+(size_t)pm*128*S_), sT=mksrd(p.ws+WS_ABT+(size_t)pn*160*8192);
  const srd_t sH=mksrd(p.ws+WS_H+(size_t)pm*48*8192), sW=mksrd(p.w2+pn*160), sHo=mksrd(p.ws+WS_H+((size_t)pm1*48+3*j)*8192);
  const unsigned r=lane>>3, cpos=(lane&7)^((lw&1)*4+(lane>>4));
  const unsigned voffA=r*(S_*2)+cpos*16, voffB=r*8192+cpos*16, voffH=(cpos>>2)*8192+r*64+(cpos&3)*16;
  unsigned voffW[5];
  #pragma unroll
  for(int i=0;i<5;i++){ const unsigned qq=i*64+lane, row=qq/20, ch=qq%20; voffW[i]=row*(D_*2)+32*((ch>>1)^((row>>3)&1))+16*(ch&1); }
  const int nY= q<2? NYA: NYB, u0= q<2? q*NYA: 2*NYA+(q-2)*NYB, o0=(q-2)*NOB, rot=(pn+pm)&3;
  constexpr int NT=NYA;
  const int nt= q<2? NYA: NYB+NOB;
  auto stageA=[&](int slot,int t){
    const unsigned lb=ldsb+slot*STG2+lw*1024u;
    if(t<nY){ const int u=u0+rot4(t,nY,rot); const unsigned ko=(u&31)*128u;
      if(u<32){
        #pragma unroll
        for(int i=0;i<4;i++) dma(sRe,voffA,(lw+4*i)*8*(S_*2)+ko,lb+i*4096u);
      } else {
        #pragma unroll
        for(int i=0;i<4;i++) dma(sIm,voffA,(lw+4*i)*8*(S_*2)+ko,lb+i*4096u);
      }
    } else { const int ko=o0+rot4(t-nY,NOB,rot);
      #pragma unroll
      for(int i=0;i<4;i++) dma_dev(sH,voffH,ko*16384u+(lw+4*i)*512u,lb+i*4096u);
    }
  };
  auto stageB=[&](int slot,int t){
    if(t<nY){ const int u=u0+rot4(t,nY,rot); const unsigned lb=ldsb+slot*STG2+O_B+lw*1024u;
      #pragma unroll
      for(int i=0;i<5;i++) dma_dev(sT,voffB,(lw+4*i)*8*8192u+u*128u,lb+i*4096u);
    } else { const int ko=o0+rot4(t-nY,NOB,rot); const unsigned lb=ldsb+slot*STG2+O_B+lw*5120u;
      #pragma unroll
      for(int i=0;i<5;i++) dma(sW,voffW[i],(ko*64u+16u*lw)*(D_*2),lb+i*1024u);
    }
  };
  (void)NT;
  stageA(0,0); stageA(1,1); stageA(2,2);
  bar();
  TS(4);
  { const LAS float* rowr=(const LAS float*)(lds+AUX)+128;
    #pragma unroll
    for(int rg=0;rg<2;rg++){ const unsigned s=(lw*2+rg)*16+(lane>>2); const float rs=rowr[s];
      const unsigned sw=((s&3)<<2)^(((s>>1)&1)|(((s>>3)&1)<<1));
      #pragma unroll
      for(int G=0;G<3;G++){ const u32x4 v=*(const LAS u32x4*)(lds+HSTG+s*256+(((4*G+(lane&3))^sw)*16)); u32x4 o;
        #pragma unroll
        for(int k=0;k<4;k++){ const float a=bflo(v[k])*rs, b=bfhi(v[k])*rs; o[k]=pk(a*sigm(a),b*sigm(b)); }
        __builtin_amdgcn_raw_buffer_store_b128(o,sHo,G*8192+s*64+(lane&3)*16,0,16); } }
  }
  TS(5);
  bar();
  stageB(0,0); stageB(1,1);
  wait_vm<5>();
  bar();
  if(lw==0 && lane==0) st_flag(p.ws,WS_HF+(pm1*16+j)*8,tag);
  stageB(2,2); stageA(3,3); stageB(3,3);
  u64 pv=0; const unsigned poff=WS_HF+(pm*16+(q&1)*8+(lane&7))*8;
  #pragma unroll 1
  for(int t=0;t<nt;t++){
    if(t==0) wait_vm<14>(); else if(t<nt-3) wait_vm<18>(); else if(t==nt-3) wait_vm<9>(); else wait_vm<0>();
    if(t==6 && q>=2 && lw==0) poll_check(pv,p.ws,poff,tag);
    bar();
    if(t==4 && q>=2 && lw==0) poll_issue(pv,p.ws,poff);
    if(t+4<nt){ stageA(t&3,t+4); stageB(t&3,t+4); }
  }
}

template<bool TR,int SLOT0,class Hook>
DI void p2_loop(LAS char* lds, int n, unsigned w, unsigned lane, f32x4 (&acc)[4][5], Hook hook){
  const unsigned wr=w>>1, wc=w&1, g=lane>>4, c16=lane&15;
  unsigned adA[2], adB[2];
  #pragma unroll
  for(int s=0;s<2;s++){ const unsigned ch=((4*s+g)^((lane>>1)&7))*16; adA[s]=(wr*64+c16)*128+ch;
    if(!TR) adB[s]=O_B+(wc*80+c16)*128+ch;
    else { const int sgn=((wc+s)&1)? -32: 32; adB[s]=O_B+320*(8*g+(c16>>2))+8*(lane&3)+160*wc+sgn*(int)(g&1); } }
  #pragma unroll
  for(int i=0;i<4;i++)
    #pragma unroll
    for(int k=0;k<5;k++) acc[i][k]=(f32x4){0.f,0.f,0.f,0.f};
  bf16x8 fa[2][4], fb[2][5]; s16x4 tl[5];
  constexpr int NRD= TR? 14: 9;
  auto rd=[&](int m,const LAS char* base,int ks,int bf){
    if(!TR){
      if(m==0) fa[bf][0]=ldf(base+adA[ks]);
      else if(m<=5) fb[bf][m-1]=ldf(base+adB[ks]+(m-1)*2048);
      else fa[bf][m-5]=ldf(base+adA[ks]+(m-5)*2048);
    } else {
      if(m==0) fa[bf][0]=ldf(base+adA[ks]);
      else if(m<=10){ const int c=(m-1)/2; const LAS char* a=base+adB[c&1]+32*c+ks*10240;
        if(m&1) tl[c]=ldtr1(a); else fb[bf][c]=cat(tl[c],ldtr1(a+1280)); }
      else fa[bf][m-10]=ldf(base+adA[ks]+(m-10)*2048);
    }
  };
  #pragma unroll
  for(int m=0;m<NRD;m++) rd(m,lds+SLOT0*STG2,0,0);
  GAP();
  #pragma unroll 1
  for(int tb=0;tb<n;tb+=4){
    #pragma unroll
    for(int u=0;u<4;u++){
      if(tb+u>=n) break;
      hook(tb+u,u);
      #pragma unroll
      for(int s=0;s<2;s++){
        const bool last=(s==1);
        wait_lgkm();
        if(last) bar();
        GAP();
        const LAS char* base=lds+((SLOT0+u+(last?1:0))&3)*STG2; const int ns=last?0:1;
        #pragma unroll
        for(int m=0;m<20;m++){ const int fm=m/5, f0=m%5, fn=(fm&1)?4-f0:f0;
          acc[fm][fn]=mma(fb[s][fn],fa[s][fm],acc[fm][fn]);
          if(m<NRD) rd(m,base,ns,s^1);
          GAP();
        }
      }
    }
  }
}
DI void put_quarter(const srd_t& s, unsigned base, unsigned lane, const f32x4 (&a)[5]){
  const u32x4 v0={pk(a[0][0],a[0][1]),pk(a[0][2],a[0][3]),pk(a[1][0],a[1][1]),pk(a[1][2],a[1][3])};
  const u32x4 v1={pk(a[2][0],a[2][1]),pk(a[2][2],a[2][3]),pk(a[3][0],a[3][1]),pk(a[3][2],a[3][3])};
  const u32x2 v2={pk(a[4][0],a[4][1]),pk(a[4][2],a[4][3])};
  __builtin_amdgcn_raw_buffer_store_b128(v0,s,base+lane*16,0,16);
  __builtin_amdgcn_raw_buffer_store_b128(v1,s,base+1024+lane*16,0,16);
  __builtin_amdgcn_raw_buffer_store_b64(v2,s,base+2048+lane*8,0,16);
}
struct Qd { u32x4 a,b; u32x2 c; };
DI Qd get_quarter(const srd_t& s, unsigned base, unsigned lane){
  Qd r; r.a=__builtin_amdgcn_raw_buffer_load_b128(s,base+lane*16,0,16); r.b=__builtin_amdgcn_raw_buffer_load_b128(s,base+1024+lane*16,0,16);
  r.c=__builtin_amdgcn_raw_buffer_load_b64(s,base+2048+lane*8,0,16); return r;
}
DI void add_quarter(float (&t)[20], const Qd& r){
  #pragma unroll
  for(int k=0;k<4;k++){ t[2*k]+=bflo(r.a[k]); t[2*k+1]+=bfhi(r.a[k]); t[8+2*k]+=bflo(r.b[k]); t[9+2*k]+=bfhi(r.b[k]); }
  #pragma unroll
  for(int k=0;k<2;k++){ t[16+2*k]+=bflo(r.c[k]); t[17+2*k]+=bfhi(r.c[k]); }
}

DI void p2_mfma(const Prm& p, LAS char* lds, int q, int pm, int pn, unsigned w, unsigned lane, u64 tag){
  const int tile=pm*4+pn;
  const int nY= q<2? NYA: NYB, u0= q<2? q*NYA: 2*NYA+(q-2)*NYB;
  if(w==1){
    const unsigned U=(u0>>1)+(lane>>2), Ul=(u0+nY-1)>>1, Uc= U<=Ul? U: Ul;
    wait_flags(p.ws,WS_ABF+((((Uc>>4)*4+pn)*16+(Uc&15))*4+(lane&3))*8,tag);
  }
  TS(5);
  bar();
  bar();
  TS(6);
  f32x4 acc[4][5];
  const srd_t sY=mksrd(p.ws+WS_YM), sX=mksrd(p.ws+WS_FX);
  if(q<2) p2_loop<false,0>(lds,NYA,w,lane,acc,[](int,int){});
  else {
    p2_loop<false,0>(lds,NYB,w,lane,acc,[](int,int){});
    TS(7);
    #pragma unroll
    for(int d=0;d<4;d++) put_quarter(sY,(((tile*2+(q-2))*4+d)*4+w)*2560,lane,acc[d]);
    p2_loop<true,(NYB&3)>(lds,NOB,w,lane,acc,[&](int t,int u){
      if(u==2 && t==2){ wait_vm<0>(); if(lane==0) st_flag(p.ws,WS_YMF+((tile*4+w)*2+(q-2))*8,tag); } });
  }
  TS(8);
  float ty[20], to[20];
  #pragma unroll
  for(int k=0;k<20;k++){ ty[k]=0.f; to[k]=0.f; }
  #pragma unroll
  for(int d=0;d<4;d++){
    if(d!=q) put_quarter(sX,(((tile*4+q)*4+d)*4+w)*2560,lane,acc[d]);
    else {
      #pragma unroll
      for(int k=0;k<20;k++){ if(q<2) ty[k]=acc[d][k>>2][k&3]; else to[k]=acc[d][k>>2][k&3]; }
    }
  }
  const unsigned wr=w>>1, wc=w&1, g=lane>>4, c16=lane&15;
  const unsigned trow=pm*128+wr*64+q*16+c16, ecol=pn*160+wc*80+4*g;
  u32x2 xv[5];
  #pragma unroll
  for(int fn=0;fn<5;fn++) xv[fn]=*(const u32x2*)(p.x+(size_t)trow*D_+ecol+16*fn);
  wait_vm<0>();
  if(lane==0) st_flag(p.ws,WS_FXF+((tile*4+w)*4+q)*8,tag);
  TS(9);
  { const unsigned l=lane<6? lane: 5;
    wait_flags(p.ws, l<4? WS_FXF+((tile*4+w)*4+l)*8 : WS_YMF+((tile*4+w)*2+(l-4))*8, tag); }
  TS(10);
  Qd r[3], y2, y3;
  #pragma unroll
  for(int k=0;k<3;k++){ const int s=(q+1+k)&3; r[k]=get_quarter(sX,(((tile*4+s)*4+q)*4+w)*2560,lane); }
  y2=get_quarter(sY,(((tile*2+0)*4+q)*4+w)*2560,lane); y3=get_quarter(sY,(((tile*2+1)*4+q)*4+w)*2560,lane);
  add_quarter(ty,y2); add_quarter(ty,y3);
  #pragma unroll
  for(int k=0;k<3;k++){ const int s=(q+1+k)&3; if(s<2) add_quarter(ty,r[k]); else add_quarter(to,r[k]); }
  #pragma unroll
  for(int fn=0;fn<5;fn++){ u32x2 o;
    #pragma unroll
    for(int k=0;k<2;k++){ const float o0=to[4*fn+2*k], o1=to[4*fn+2*k+1];
      o[k]=pk(bflo(xv[fn][k])+ty[4*fn+2*k]*(o0*sigm(o0)), bfhi(xv[fn][k])+ty[4*fn+2*k+1]*(o1*sigm(o1))); }
    *(u32x2*)(p.out+(size_t)trow*D_+ecol+16*fn)=o; }
  TS(11);
}

__global__ __launch_bounds__(512,2) void fnet_mlp_fused(Prm p){
  extern __shared__ __attribute__((aligned(1024))) char smem[];
  LAS char* lds=(LAS char*)smem;
  const unsigned tid=threadIdx.x, lane=tid&63, w=rfl(tid>>6);
  const int b=blockIdx.x, x8=b&7, r=b>>3;
  const int pm1=x8*2+(r>>4), j=r&15;
  const int q=x8>>1, pm2=(x8&1)*8+(r>>2), pn=r&3;
  TS(0);
  if(w>=4){
    const unsigned lw=w-4;
    const u64 tag= lw<2? p1_loader<3>(p,lds,pm1,j,lw,lane) : p1_loader<2>(p,lds,pm1,j,lw,lane);
    TS(3);
    p2_loader(p,lds,pm1,j,q,pm2,pn,lw,lane,tag);
    TS(8);
    if(b==0 && lw==0){
      #pragma unroll 1
      for(int i=0;i<4;i++) wait_flags(p.ws,WS_SF1+(i*64+lane)*32,tag);
      if(lane==0) st_flag(p.ws,0,tag);
    }
  } else {
    const u64 tag= (w&1)? p1_mfma<1>(p,lds,pm1,j,w,lane) : p1_mfma<0>(p,lds,pm1,j,w,lane);
    p2_mfma(p,lds,q,pm2,pn,w,lane,tag);
  }
}

extern "C" void kernel_launch(void* const* d_in, const int* in_sizes, int n_in, void* d_out, int out_size, void* d_ws, size_t ws_size, hipStream_t stream) {
  Prm p; p.x=(const u16*)d_in[0]; p.wsre=(const u16*)d_in[1]; p.wsim=(const u16*)d_in[2]; p.wdre=(const u16*)d_in[3]; p.wdim=(const u16*)d_in[4];
  p.w1=(const u16*)d_in[6]; p.w2=(const u16*)d_in[7]; p.out=(u16*)d_out; p.ws=(char*)d_ws;
  (void)hipFuncSetAttribute((const void*)fnet_mlp_fused, hipFuncAttributeMaxDynamicSharedMemorySize, LDS_BYTES);
  fnet_mlp_fused<<<256,512,LDS_BYTES,stream>>>(p);
}
```

```cpp
#include <hip/hip_runtime.h>
#include <stdint.h>
#include <type_traits>
#include <utility>
typedef unsigned short u16;
typedef unsigned long long u64;
typedef float f32x4 __attribute__((ext_vector_type(4)));
typedef float f32x2 __attribute__((ext_vector_type(2)));
typedef __bf16 bf16x8 __attribute__((ext_vector_type(8)));
typedef __bf16 bf16x2 __attribute__((ext_vector_type(2)));
typedef short s16x4 __attribute__((ext_vector_type(4)));
typedef short s16x8 __attribute__((ext_vector_type(8)));
typedef unsigned u32x4 __attribute__((ext_vector_type(4)));
typedef unsigned u32x2 __attribute__((ext_vector_type(2)));
#define DI __device__ __forceinline__
#define GAP() __builtin_amdgcn_sched_barrier(0)
#define LAS __attribute__((address_space(3)))
#define GAS __attribute__((address_space(1)))
typedef GAS u64 gu64;
typedef GAS unsigned gu32;
typedef __amdgpu_buffer_rsrc_t srd_t;

constexpr int S_=2048, D_=640, F_=1536;
constexpr unsigned WS_ABF=8192, WS_HF=12288, WS_YF=16384, WS_OF=24576, WS_RST=65536;
constexpr unsigned WS_ABT=1u<<20, WS_H=8u<<20, WS_YX=16u<<20, WS_FX=28u<<20;
constexpr int STG1=28672, O_W=16384, ABS=4*STG1, AUX=155648, ROWM=AUX, ROWR=AUX+512, CSA=AUX+1024, CSH=AUX+2048, STG=AUX+4096, LDS_BYTES=163840;
constexpr int slot1(int i){ return (i<10? i%5: (i-10)%4)*STG1; }
constexpr int PA[4]={114688,131072,98304,20480}, PB[4]={57344,77824,0,36864}, HSTG=20480;
constexpr unsigned SPIN_MAX=40000;
#define ROTEXPR ((pn+pm)&3)
#define K0EXPR (5*((pn+pm)&1))
constexpr int NY=16, NO=6, NT=NY+NO;

#define TS(i)
#define TT(ph,t)
#define TSR(l)
#define TSPUT()
#define TSP
#define TSA

template<int I> using ic=std::integral_constant<int,I>;
template<int... I,class F> DI void sfor_(std::integer_sequence<int,I...>, F&& f){ (f(ic<I>{}),...); }
template<int N,class F> DI void sfor(F&& f){ sfor_(std::make_integer_sequence<int,N>{},f); }
DI unsigned rfl(unsigned x){ return __builtin_amdgcn_readfirstlane(x); }
DI float bflo(unsigned v){ return __uint_as_float(v<<16); }
DI float bfhi(unsigned v){ return __uint_as_float(v&0xffff0000u); }
DI unsigned pk(float lo,float hi){ f32x2 v={lo,hi}; bf16x2 b=__builtin_convertvector(v,bf16x2); return __builtin_bit_cast(unsigned,b); }
DI float sigm(float v){ return __builtin_amdgcn_rcpf(1.f+__builtin_amdgcn_exp2f(-1.4426950408889634f*v)); }
template<int N> DI void wait_vm(){ asm volatile("s_waitcnt vmcnt(%0)"::"n"(N):"memory"); }
DI void bar(){ __builtin_amdgcn_s_barrier(); }
DI void wait_lgkm(){ __builtin_amdgcn_s_waitcnt(0xC07F); }
constexpr unsigned BIAS=4096;
#define DMA1(O,X) "buffer_load_dwordx4 %" #O ", %1, %2 offen" X " lds"
#define DMA_GROUP(X) \
  if constexpr(N==1) asm volatile("s_mov_b32 m0, %0\n\ts_nop 0\n\tbuffer_load_dwordx4 %3, %1, %2 offen" X " lds" :: "s"(m0v),"s"(srd),"s"(soff),"v"(v[0]) : "memory"); \
  if constexpr(N==2) asm volatile("s_mov_b32 m0, %0\n\ts_nop 0\n\tbuffer_load_dwordx4 %3, %1, %2 offen" X " lds\n\tbuffer_load_dwordx4 %4, %1, %2 offen offset:1024" X " lds" :: "s"(m0v),"s"(srd),"s"(soff),"v"(v[0]),"v"(v[1]) : "memory"); \
  if constexpr(N==3) asm volatile("s_mov_b32 m0, %0\n\ts_nop 0\n\tbuffer_load_dwordx4 %3, %1, %2 offen" X " lds\n\tbuffer_load_dwordx4 %4, %1, %2 offen offset:1024" X " lds\n\tbuffer_load_dwordx4 %5, %1, %2 offen offset:2048" X " lds" :: "s"(m0v),"s"(srd),"s"(soff),"v"(v[0]),"v"(v[1]),"v"(v[2]) : "memory"); \
  if constexpr(N==4) asm volatile("s_mov_b32 m0, %0\n\ts_nop 0\n\tbuffer_load_dwordx4 %3, %1, %2 offen" X " lds\n\tbuffer_load_dwordx4 %4, %1, %2 offen offset:1024" X " lds\n\tbuffer_load_dwordx4 %5, %1, %2 offen offset:2048" X " lds\n\tbuffer_load_dwordx4 %6, %1, %2 offen offset:3072" X " lds" :: "s"(m0v),"s"(srd),"s"(soff),"v"(v[0]),"v"(v[1]),"v"(v[2]),"v"(v[3]) : "memory");
#define SC1S " sc1"
template<int N,bool DEV> DI void dma_group(const __amdgpu_buffer_rsrc_t& srd, const unsigned* v, unsigned soff, unsigned m0v){
  if constexpr(DEV){ DMA_GROUP(SC1S) } else { DMA_GROUP("") }
}
template<bool DEV> DI void dma5(const __amdgpu_buffer_rsrc_t& srd, const unsigned* v, unsigned soff, unsigned m0v){
  dma_group<4,DEV>(srd,v,soff,m0v); dma_group<1,DEV>(srd,v+4,soff,m0v+4096u);
}
DI srd_t mksrd(const void* p){ return __builtin_amdgcn_make_buffer_rsrc((void*)p,0,0x7ffffff0,0x00020000); }
DI srd_t mksrd_lo(const void* p){ return mksrd((const char*)p-BIAS); }
DI void st_flag(char* ws, unsigned off, u64 tag){ __hip_atomic_store((gu64*)(ws+off), tag, __ATOMIC_RELAXED, __HIP_MEMORY_SCOPE_AGENT); }
DI void st_f32(char* ws, unsigned off, float v){ __hip_atomic_store((gu32*)(ws+off), __float_as_uint(v), __ATOMIC_RELAXED, __HIP_MEMORY_SCOPE_AGENT); }
DI u64 ld_flag(char* ws, unsigned off){ return __hip_atomic_load((gu64*)(ws+off), __ATOMIC_RELAXED, __HIP_MEMORY_SCOPE_AGENT); }
DI void wait_flags(char* ws, unsigned off, u64 tag){
  #pragma unroll 1
  for(unsigned s=0;s<SPIN_MAX;s++){ const u64 v=ld_flag(ws,off); if(__all(v==tag)) break; __builtin_amdgcn_s_sleep(2); }
}
DI u64 get_tag(const char* ws){ u64 ep; asm volatile("s_load_dwordx2 %0, %1, 0x0\n\ts_waitcnt lgkmcnt(0)":"=s"(ep):"s"(ws):"memory"); return ep+1; }
DI void poll_issue(u64& v, char* ws, unsigned off){ const char* a=ws+off; asm volatile("global_load_dwordx2 %0, %1, off sc1":"=v"(v):"v"(a):"memory"); }
DI void poll_check(u64& v, char* ws, unsigned off, u64 tag){ asm volatile("":"+v"(v)); if(!__all(v==tag)) wait_flags(ws,off,tag); }
DI bf16x8 ldf(const LAS char* a){ return *(const LAS bf16x8*)a; }
DI bf16x8 ldtr(const LAS char* a, int step){
  const s16x4 lo=__builtin_amdgcn_ds_read_tr16_b64_v4i16((LAS s16x4*)a), hi=__builtin_amdgcn_ds_read_tr16_b64_v4i16((LAS s16x4*)(a+step));
  return __builtin_bit_cast(bf16x8,(s16x8)__builtin_shufflevector(lo,hi,0,1,2,3,4,5,6,7));
}
DI s16x4 ldtr1(const LAS char* a){ return __builtin_amdgcn_ds_read_tr16_b64_v4i16((LAS s16x4*)a); }
DI bf16x8 cat(s16x4 lo, s16x4 hi){ return __builtin_bit_cast(bf16x8,(s16x8)__builtin_shufflevector(lo,hi,0,1,2,3,4,5,6,7)); }
DI f32x4 mma(bf16x8 a, bf16x8 b, f32x4 c){ return __builtin_amdgcn_mfma_f32_16x16x32_bf16(a,b,c,0,0,0); }

template<int CTRL> DI float dpp_add(float v){ return v+__int_as_float(__builtin_amdgcn_update_dpp(0,__float_as_int(v),CTRL,0xf,0xf,true)); }
DI float sum16(float v){ v=dpp_add<0xB1>(v); v=dpp_add<0x4E>(v); v=dpp_add<0x141>(v); return dpp_add<0x140>(v); }

struct Prm { const u16* x; const u16* wsre; const u16* wsim; const u16* wdre; const u16* wdim; const u16* w1; const u16* w2; u16* out; char* ws; };

enum { K_P1, K_YA, K_YB, K_O, K_STAT, K_STCHK, K_ABS, K_FAB, K_PAB, K_CAB, K_HRD, K_HS, K_FH };
struct Op { int k, i, step; };
struct Sched { Op op[160]; int n; };
constexpr int TB1=20, TP2=21, TEND=TP2+NT;
#define ST_FAB 14
#define ST_FH 8
#define T_PH 10
constexpr Sched make_sched(){
  Sched s{}; auto add=[&](int k,int i,int st){ s.op[s.n].k=k; s.op[s.n].i=i; s.op[s.n].step=st; s.n++; };
  add(K_P1,0,-2); add(K_P1,1,-2); add(K_P1,2,-1); add(K_P1,3,-1); add(K_P1,4,-1);
  for(int st=0;st<20;st++){
    if(st==4) add(K_STAT,0,st);
    if(st==ST_FAB) add(K_FAB,0,st);
    if(st<=8) add(K_P1,st+5,st); else if(st>=10 && st<=15) add(K_P1,st+4,st);
    if(st==8) add(K_STCHK,0,st);
    if(st==10) add(K_ABS,0,st);
    if(st==15) add(K_YA,0,st);
    if(st==16) add(K_YA,1,st);
    if(st==18) add(K_PAB,0,st);
    if(st==19){ add(K_CAB,0,st); add(K_YB,0,st); add(K_YB,1,st); add(K_YA,2,st); add(K_YB,2,st); }
  }
  add(K_HRD,0,TB1);
  for(int t=0;t<NT;t++){ const int st=TP2+t;
    if(t==ST_FH) add(K_FH,0,st);
    if(t==0){ add(K_YA,3,st); add(K_YB,3,st); }
    if(t<6) add(K_HS,t,st);
    if(t+4<NY){ add(K_YA,t+4,st); add(K_YB,t+4,st); } else if(t+4<NT) add(K_O,t+4,st);
  }
  return s;
}
constexpr Sched SCH=make_sched();
constexpr int op_cnt(const Op& o,int nwd){ return o.k==K_P1? (o.i<10? 4+nwd: 7): o.k==K_YA? 4: o.k==K_YB? 5: o.k==K_O? 9: o.k==K_ABS? 5: (o.k==K_HS||o.k==K_PAB)? 1: 0; }
constexpr bool needed(const Op& o,int tau){
  if(tau<19 && o.k==K_P1 && o.i==tau+1) return true;
  if(tau==8 && o.k==K_STAT) return true;
  if(tau>=TB1 && tau<TEND-1){ const int t=tau-TB1; if((o.k==K_YA||o.k==K_YB||o.k==K_O) && o.i==t) return true; }
  return false; }
constexpr int last_needed(int tau){ int l=-1; for(int n=0;n<SCH.n;n++) if(SCH.op[n].step<tau && needed(SCH.op[n],tau)) l=n; return l; }
constexpr int allowed(int tau,int nwd){ const int l=last_needed(tau); if(l<0) return -1; int c=0; for(int n=l+1;n<SCH.n;n++) if(SCH.op[n].step<tau) c+=op_cnt(SCH.op[n],nwd); return c; }
constexpr int find_op(int k,int i){ for(int n=0;n<SCH.n;n++) if(SCH.op[n].k==k && SCH.op[n].i==i) return n; return 1<<20; }
constexpr bool done_by(int k,int i,int tau){ const int p=find_op(k,i); for(int t=-1;t<=tau;t++) if(last_needed(t)>=p) return true; return false; }
constexpr int step_begin(int st){ int n=0; while(n<SCH.n && SCH.op[n].step<st) n++; return n; }
static_assert(done_by(K_ABS,0,ST_FAB) && done_by(K_HS,5,TP2+ST_FH),"a flag follows its completed stores");
static_assert(T_PH<NY-4,"schedule");

template<int NWD>
DI u64 loader(const Prm& p, LAS char* lds, int pm, int j, int q, int pn, unsigned lw, unsigned lane TSP){
  const unsigned ldsb=(unsigned)(size_t)lds;
  const unsigned r=lane>>3;
  const int rot=ROTEXPR, k0=K0EXPR;
  u64 tag, pv=0;
  const srd_t sA=mksrd_lo(p.x+(size_t)pm*128*D_), sD=mksrd_lo((j<8? p.wdre: p.wdim)+(size_t)(j&7)*80*D_), sW=mksrd_lo(p.w1+j*96);
  const unsigned q0= NWD==3? 3*lw: 2*lw+2;
  unsigned vX[4], vD[NWD], vW[3];
  #pragma unroll
  for(int i=0;i<4;i++) vX[i]=(32*lw+16*(i&1)+4*(i>>1)+8*(r>>2)+(r&3))*(D_*2)+((lane&7)^((i&1)*4+(r>>1)))*16+BIAS-1024*i;
  #pragma unroll
  for(int i=0;i<NWD;i++) vD[i]=((q0+i)*8+r)*(D_*2)+((lane&7)^(((q0+i)&1)*4+(r>>1)))*16+BIAS-1024*i;
  #pragma unroll
  for(int i=0;i<3;i++){ const unsigned c=i*64+lane, row=c/12, ch=c%12; vW[i]=(16*lw+row)*(F_*2)+32*((ch>>1)^((row>>3)&1))+16*(ch&1)+BIAS-1024*i; }
  auto p1=[&](auto I){ constexpr int i=I.value; int kt=k0+(i%10); kt= kt>=10? kt-10: kt;
    dma_group<4,false>(sA,vX,kt*128u,ldsb+lw*4096u+slot1(i));
    if constexpr(i<10) dma_group<NWD,false>(sD,vD,kt*128u,ldsb+O_W+q0*1024u+slot1(i));
    else dma_group<3,false>(sW,vW,kt*(64u*F_*2),ldsb+O_W+lw*3072u+slot1(i)); };
  TS(1);
  p1(ic<0>{}); p1(ic<1>{});
  TS(2);
  tag=get_tag(p.ws);
  const srd_t sWs=mksrd_lo((q<2? p.wsre: p.wsim)+(size_t)pm*128*S_+(q&1)*1024), sT=mksrd_lo(p.ws+WS_ABT+(size_t)pn*160*8192+q*2048);
  const srd_t sH=mksrd_lo(p.ws+WS_H+((size_t)pm*48+q*12)*8192), sW2=mksrd_lo(p.w2+(size_t)q*384*D_+pn*160), sWk=mksrd(p.ws);
  unsigned vA[4], vB[5], vH[4], vW2[5];
  #pragma unroll
  for(int i=0;i<4;i++){ const unsigned c=(lane&7)^((i&1)*4+(r>>1));
    vA[i]=(32*lw+8*i+r)*(S_*2)+c*16+BIAS-1024*i; vH[i]=(c>>2)*8192+(32*lw+8*i+r)*64+(c&3)*16+BIAS-1024*i; }
  #pragma unroll
  for(int i=0;i<5;i++){ const unsigned bi=BIAS-(i<4? 1024*i: 0);
    vB[i]=((5*lw+i)*8+r)*8192+((lane&7)^(((lw+i)&1)*4+(r>>1)))*16+bi;
    const unsigned qq=i*64+lane, row=qq/20, ch=qq%20; vW2[i]=(16*lw+row)*(D_*2)+32*((ch>>1)^((row>>3)&1))+16*(ch&1)+bi; }
  const unsigned lA2=ldsb+lw*4096u, lB2=ldsb+lw*5120u;
  u32x4 hv[6];
  const unsigned pAB=WS_ABF+((((q>>1)*4+pn)*16+(q&1)*8)*2+(lane&15))*8;
  auto exec=[&](auto N){ constexpr Op o=SCH.op[N.value];
    if constexpr(o.k==K_P1) p1(ic<o.i>{});
    if constexpr(o.k==K_YA){ const int u=(o.i&~3)|((o.i+rot)&3); dma_group<4,false>(sWs,vA,u*128u,lA2+PA[o.i&3]); }
    if constexpr(o.k==K_YB){ const int u=(o.i&~3)|((o.i+rot)&3); dma5<true>(sT,vB,u*128u,lB2+PB[o.i&3]); }
    if constexpr(o.k==K_O){ int ko=o.i-NY+rot; ko= ko>=NO? ko-NO: ko;
      dma_group<4,true>(sH,vH,ko*16384u,lA2+PA[o.i&3]); dma5<false>(sW2,vW2,ko*(64u*D_*2),lB2+PB[o.i&3]); }
    if constexpr(o.k==K_STAT){ if(NWD==3 && lw==0){ const unsigned v0=WS_RST+pm*2048+lane*16, vs[2]={v0,v0}; dma_group<2,true>(sWk,vs,0,ldsb+STG); } }
    if constexpr(o.k==K_STCHK){ if(NWD==3 && lw==0){
        u32x4 a=*(const LAS u32x4*)(lds+STG+lane*16), b=*(const LAS u32x4*)(lds+STG+1024+lane*16);
        const unsigned tg=(unsigned)tag;
        if(!__all(a[1]==tg && a[3]==tg && b[1]==tg && b[3]==tg)){
          #pragma unroll 1
          for(unsigned sp=0;sp<SPIN_MAX;sp++){ asm volatile("":::"memory");
            a=__builtin_amdgcn_raw_buffer_load_b128(sWk,WS_RST+pm*2048+lane*16,0,16); b=__builtin_amdgcn_raw_buffer_load_b128(sWk,WS_RST+pm*2048+1024+lane*16,0,16);
            if(__all(a[1]==tg && a[3]==tg && b[1]==tg && b[3]==tg)) break; } }
        LAS unsigned* rm=(LAS unsigned*)(lds+ROWM); LAS unsigned* rr=(LAS unsigned*)(lds+ROWR);
        rm[lane]=a[0]; rr[lane]=a[2]; rm[64+lane]=b[0]; rr[64+lane]=b[2];
        wait_lgkm(); } }
    if constexpr(o.k==K_ABS){
      u32x4 v[5];
      #pragma unroll
      for(int n=0;n<5;n++){ const unsigned col=20*lw+4*n+(lane>>4); v[n]=*(const LAS u32x4*)(lds+ABS+col*256+(((lane&15)^(col&15))*16)); }
      #pragma unroll
      for(int n=0;n<5;n++){ const unsigned col=20*lw+4*n+(lane>>4);
        __builtin_amdgcn_raw_buffer_store_b128(v[n],sWk,WS_ABT+((j&7)*80+col)*8192+(j>>3)*4096+pm*256+(lane&15)*16,0,16); } }
    if constexpr(o.k==K_FAB){ if(lw==0 && lane==0) st_flag(p.ws,WS_ABF+((((j>>3)*4+((j&7)>>1))*16+pm)*2+(j&1))*8,tag); }
    if constexpr(o.k==K_PAB) poll_issue(pv,p.ws,pAB);
    if constexpr(o.k==K_CAB){ wait_vm<0>(); poll_check(pv,p.ws,pAB,tag); }
    if constexpr(o.k==K_HRD){
      #pragma unroll
      for(int rg=0;rg<2;rg++){ const unsigned s=(lw*2+rg)*16+(lane>>2), sw=((s&3)<<2)^(((s>>1)&1)|(((s>>3)&1)<<1));
        #pragma unroll
        for(int G=0;G<3;G++) hv[rg*3+G]=*(const LAS u32x4*)(lds+HSTG+s*256+(((4*G+(lane&3))^sw)*16)); }
      wait_lgkm(); }
    if constexpr(o.k==K_HS) __builtin_amdgcn_raw_buffer_store_b128(hv[o.i],sWk,WS_H+(pm*48+3*j+o.i%3)*8192+((lw*2+o.i/3)*16+(lane>>2))*64+(lane&3)*16,0,16);
    if constexpr(o.k==K_FH){ if(lw==0 && lane==0) st_flag(p.ws,WS_HF+(pm*16+j)*8,tag); }
  };
  sfor<TEND+1>([&](auto I){ constexpr int tau=I.value-1, al=allowed(tau,NWD);
    static_assert(al<64,"vmcnt range");
    if constexpr(al>=0) wait_vm<al>();
    if constexpr(tau==-1) TS(3);
    bar();
    if constexpr(tau==-1) TS(5); else if constexpr(tau<TB1) TT(0,tau); else if constexpr(tau==TB1) TS(4); else TT(1,tau-TP2);
    constexpr int b0=step_begin(tau), b1=step_begin(tau+1);
    sfor<b1-b0>([&](auto M){ exec(ic<b0+M.value>{}); });
  });
  return tag;
}
DI u64 p1_mfma(const Prm& p, LAS char* lds, int pm, int j, unsigned w, unsigned lane TSP){
  u64 tag;
  const unsigned wr=w>>1, wc=w&1, g=lane>>4, c16=lane&15;
  if(w<2){ const unsigned row=pm*128+j*8+w*4+g; u32x4 v[5]; float s=0;
    #pragma unroll
    for(int i=0;i<5;i++) v[i]=*(const u32x4*)(p.x+(size_t)row*D_+(c16+16*i)*8);
    tag=get_tag(p.ws);
    #pragma unroll
    for(int i=0;i<5;i++)
      #pragma unroll
      for(int k=0;k<4;k++) s+=bflo(v[i][k])+bfhi(v[i][k]);
    const float m=sum16(s)*(1.f/D_); float q2=0;
    #pragma unroll
    for(int i=0;i<5;i++)
      #pragma unroll
      for(int k=0;k<4;k++){ const float a=bflo(v[i][k])-m, c=bfhi(v[i][k])-m; q2+=a*a+c*c; }
    const float rs=1.0f/sqrtf(sum16(q2)*(1.f/D_)+1e-5f);
    if(c16<2) st_flag(p.ws,WS_RST+row*16+c16*8, ((u64)(unsigned)tag<<32)|__float_as_uint(c16==0? m: rs));
  } else tag=get_tag(p.ws);
  TS(1);
  const u32x4 one4={0x3F803F80u,0x3F803F80u,0x3F803F80u,0x3F803F80u}; const bf16x8 ones=__builtin_bit_cast(bf16x8,one4);
  const unsigned rho=3*wr;
  unsigned aA[2], aB[2][5], hA[2], hB[2][3];
  #pragma unroll
  for(int s=0;s<2;s++){
    { const unsigned ch=((4*(s^wc)+g)^((lane>>1)&7))*16; aA[s]=(32*w+c16)*128+ch;
      #pragma unroll
      for(int f=0;f<5;f++){ const unsigned fr=(f+rho)%5; aB[s][f]=O_W+(fr*16+c16)*128+ch; } }
    { const unsigned ks=s^wr, ch=((4*ks+g)^((lane>>1)&7))*16; hA[s]=(wr*64+c16)*128+ch;
      #pragma unroll
      for(int f=0;f<3;f++) hB[s][f]=O_W+192*(8*g+(c16>>2))+8*(lane&3)+32*((3*wc+f)^(g&1))+ks*6144; }
  }
  f32x4 acc[2][5], csa[3];
  #pragma unroll
  for(int k=0;k<5;k++) acc[0][k]=acc[1][k]=(f32x4){0.f,0.f,0.f,0.f};
  csa[0]=csa[1]=csa[2]=(f32x4){0.f,0.f,0.f,0.f};
  bf16x8 fa[2][2], fb[2][5], ga[2][4], gb[2][3]; s16x4 tl[3];
  auto rdA=[&](int m,const LAS char* base,int s){
    if(m==0) fa[s][0]=ldf(base+aA[s]); else if(m<=5) fb[s][m-1]=ldf(base+aB[s][m-1]); else fa[s][1]=ldf(base+aA[s]+2048); };
  auto rdH=[&](int m,const LAS char* base,int s){
    if(m==0) ga[s][0]=ldf(base+hA[s]);
    else if(m<=6){ const int c=(m-1)/2; if(m&1) tl[c]=ldtr1(base+hB[s][c]); else gb[s][c]=cat(tl[c],ldtr1(base+hB[s][c]+768)); }
    else ga[s][m-6]=ldf(base+hA[s]+(m-6)*2048); };
  bar();
  TS(2);
  #pragma unroll
  for(int m=0;m<7;m++) rdA(m,lds,0);
  GAP();
  const unsigned s0=32*w+8*g;
  f32x4 m0,m1,r0,r1; float c[5];
  sfor<10>([&](auto T){ constexpr int t=T.value;
    #pragma unroll
    for(int s=0;s<2;s++){
      const bool last=(s==1);
      if(last && t==9){ LAS float* cw=(LAS float*)(lds+CSA)+wc*80;
        if(g==0){ cw[rho*16+c16]=csa[0][0]; cw[(rho+1)*16+c16]=csa[1][0]; if(wr==0) cw[32+c16]=csa[2][0]; } }
      wait_lgkm();
      if(last) bar();
      GAP();
      const LAS char* base=lds+slot1(last? t+1: t);
      if(last && t==9){ const LAS float* cs=(const LAS float*)(lds+CSA);
        #pragma unroll
        for(int f=0;f<5;f++){ const unsigned fr=(f+rho)%5; c[f]=cs[fr*16+c16]+cs[80+fr*16+c16]; }
        m0=*(const LAS f32x4*)(lds+ROWM+s0*4); m1=*(const LAS f32x4*)(lds+ROWM+s0*4+16); r0=*(const LAS f32x4*)(lds+ROWR+s0*4); r1=*(const LAS f32x4*)(lds+ROWR+s0*4+16); }
      constexpr int NM= 13;
      #pragma unroll
      for(int m=0;m<NM;m++){
        if(!last){ if(m<3) csa[m]=mma(ones,fb[s][m],csa[m]); else { const int mm=m-3, fm=mm/5, f0=mm%5, fn=fm?4-f0:f0; acc[fm][fn]=mma(fa[s][fm],fb[s][fn],acc[fm][fn]); } }
        else if(m<10){ const int fm=m/5, f0=m%5, fn=fm?4-f0:f0; acc[fm][fn]=mma(fa[s][fm],fb[s][fn],acc[fm][fn]); }
        if(last && t==9){ if(m<10) rdH(m,base,0); }
        else if(m<7) rdA(m,base,s^1);
        GAP();
      }
    }
  });
  TS(3);
  { const float sg= j<8? 1.f: -1.f;
    r0*=sg; r1*=sg;
    #pragma unroll
    for(int f=0;f<5;f++){ const unsigned col=((f+rho)%5)*16+c16;
      const f32x4 a=(acc[0][f]-m0*c[f])*r0, b=(acc[1][f]-m1*c[f])*r1;
      const u32x4 o={pk(a[0],a[1]),pk(a[2],a[3]),pk(b[0],b[1]),pk(b[2],b[3])};
      *(LAS u32x4*)(lds+ABS+col*256+(((4*w+g)^c16)*16))=o; }
  }
  GAP();
  TS(4);
  f32x4 hac[4][3], csh[3], c4[3];
  #pragma unroll
  for(int k=0;k<3;k++){ csh[k]=(f32x4){0.f,0.f,0.f,0.f};
    #pragma unroll
    for(int i=0;i<4;i++) hac[i][k]=(f32x4){0.f,0.f,0.f,0.f}; }
  float hm[4], hr[4]; unsigned hb[4], hx[4];
  sfor<10>([&](auto T){ constexpr int t=T.value, i=10+t;
    #pragma unroll
    for(int s=0;s<2;s++){
      const bool last=(s==1);
      if(last && t==9){ LAS float* cw=(LAS float*)(lds+CSH)+wr*96+wc*48;
        if(c16==0){
          #pragma unroll
          for(int f=0;f<3;f++) *(LAS f32x4*)(cw+16*f+4*g)=csh[f]; } }
      wait_lgkm();
      if(last) bar();
      GAP();
      const LAS char* base=lds+slot1(last? (t==9? i: i+1): i);
      if(last && t==9){ const LAS float* cs=(const LAS float*)(lds+CSH)+wc*48;
        #pragma unroll
        for(int f=0;f<3;f++) c4[f]=*(const LAS f32x4*)(cs+16*f+4*g)+*(const LAS f32x4*)(cs+96+16*f+4*g);
        #pragma unroll
        for(int fm=0;fm<4;fm++){ const unsigned s=wr*64+32*(fm>>1)+16*(c16>>3)+8*((c16>>2)&1)+4*(fm&1)+(c16&3);
          hm[fm]=((const LAS float*)(lds+ROWM))[s]; hr[fm]=((const LAS float*)(lds+ROWR))[s];
          hb[fm]=HSTG+s*256+(g&1)*8; hx[fm]=((g>>1)^((s&3)<<2)^(((s>>1)&1)|(((s>>3)&1)<<1)))*16; } }
      #pragma unroll
      for(int m=0;m<15;m++){
        if(!last){ if(m<3) csh[m]=mma(gb[s][m],ones,csh[m]); else { const int mm=m-3, fm=mm/3, f0=mm%3, fn=(fm&1)?2-f0:f0; hac[fm][fn]=mma(gb[s][fn],ga[s][fm],hac[fm][fn]); } }
        else if(m<12){ const int fm=m/3, f0=m%3, fn=(fm&1)?2-f0:f0; hac[fm][fn]=mma(gb[s][fn],ga[s][fm],hac[fm][fn]); }
        if(m<10 && !(last && t==9)) rdH(m,base,s^1);
        GAP();
      }
    }
  });
  TS(5);
  #pragma unroll
  for(int fm=0;fm<4;fm++){ const float zr=hr[fm]*-1.4426950408889634f;
    #pragma unroll
    for(int f=0;f<3;f++){ const f32x4 v=hac[fm][f]-c4[f]*hm[fm], a=v*hr[fm], z=v*zr; f32x4 e;
      #pragma unroll
      for(int k=0;k<4;k++) e[k]=__builtin_amdgcn_exp2f(z[k]);
      e+=1.f;
      #pragma unroll
      for(int k=0;k<4;k++) e[k]=__builtin_amdgcn_rcpf(e[k]);
      const f32x4 h=a*e; const u32x2 o={pk(h[0],h[1]),pk(h[2],h[3])};
      *(LAS u32x2*)(lds+hb[fm]+(hx[fm]^((3*wc+f)*32)))=o; } }
  wait_lgkm();
  bar();
  TS(6);
  return tag;
}

template<bool TR,int T0,int N,class Hook>
DI void p2_loop(LAS char* lds, int q, unsigned w, unsigned lane, f32x4 (&acc)[4][5], Hook hook){
  const unsigned wr=w>>1, wc=w&1, g=lane>>4, c16=lane&15;
  unsigned adA[2][4], adB[2];
  #pragma unroll
  for(int s=0;s<2;s++){ const unsigned ch=((4*s+g)^((lane>>1)&7))*16;
    #pragma unroll
    for(int fm=0;fm<4;fm++) adA[s][fm]=(wr*64+((fm+q)&3)*16+c16)*128+ch;
    if(!TR) adB[s]=(wc*80+c16)*128+ch;
    else { const int sgn=((wc+s)&1)? -32: 32; adB[s]=320*(8*g+(c16>>2))+8*(lane&3)+160*wc+sgn*(int)(g&1); } }
  #pragma unroll
  for(int i=0;i<4;i++)
    #pragma unroll
    for(int k=0;k<5;k++) acc[i][k]=(f32x4){0.f,0.f,0.f,0.f};
  bf16x8 fa[2][4], fb[2][5]; s16x4 tl[5];
  constexpr int NRD= TR? 14: 9;
  auto rd=[&](int m,int slot,int ks,int bf){
    const LAS char* bA=lds+PA[slot]; const LAS char* bB=lds+PB[slot];
    if(!TR){
      if(m==0) fa[bf][0]=ldf(bA+adA[ks][0]);
      else if(m<=5) fb[bf][m-1]=ldf(bB+adB[ks]+(m-1)*2048);
      else fa[bf][m-5]=ldf(bA+adA[ks][m-5]);
    } else {
      if(m==0) fa[bf][0]=ldf(bA+adA[ks][0]);
      else if(m<=10){ const int c=(m-1)/2; const LAS char* a=bB+adB[c&1]+32*c+ks*10240;
        if(m&1) tl[c]=ldtr1(a); else fb[bf][c]=cat(tl[c],ldtr1(a+1280)); }
      else fa[bf][m-10]=ldf(bA+adA[ks][m-10]);
    }
  };
  #pragma unroll
  for(int m=0;m<NRD;m++) rd(m,T0&3,0,0);
  GAP();
  sfor<N>([&](auto T){ constexpr int t=T.value;
    sfor<2>([&](auto Sx){ constexpr int s=Sx.value; constexpr bool last=(s==1);
      wait_lgkm();
      if(last){ hook(T,ic<-1>{}); bar(); }
      GAP();
      constexpr int slot=(T0+t+(last?1:0))&3, ns=last?0:1;
      sfor<20>([&](auto M){ constexpr int m=M.value, fm=m/5, f0=m%5, fn=(fm&1)?4-f0:f0;
        acc[fm][fn]=mma(fb[s][fn],fa[s][fm],acc[fm][fn]);
        if(m<NRD && !(last && t==N-1)) rd(m,slot,ns,s^1);
        hook(T,ic<s*20+m>{});
        GAP();
      });
    });
  });
}
struct Qd { u32x4 a,b; u32x2 c; };
DI Qd pack_quarter(const f32x4 (&a)[5]){ Qd r;
  r.a=(u32x4){pk(a[0][0],a[0][1]),pk(a[0][2],a[0][3]),pk(a[1][0],a[1][1]),pk(a[1][2],a[1][3])};
  r.b=(u32x4){pk(a[2][0],a[2][1]),pk(a[2][2],a[2][3]),pk(a[3][0],a[3][1]),pk(a[3][2],a[3][3])};
  r.c=(u32x2){pk(a[4][0],a[4][1]),pk(a[4][2],a[4][3])}; return r; }
template<int PART> DI void put_part(const srd_t& s, unsigned base, unsigned lane, const Qd& v){
  if constexpr(PART==0) __builtin_amdgcn_raw_buffer_store_b128(v.a,s,base+lane*16,0,16);
  if constexpr(PART==1) __builtin_amdgcn_raw_buffer_store_b128(v.b,s,base+1024+lane*16,0,16);
  if constexpr(PART==2) __builtin_amdgcn_raw_buffer_store_b64(v.c,s,base+2048+lane*8,0,16); }
DI Qd get_quarter(const srd_t& s, unsigned base, unsigned lane){
  Qd r; r.a=__builtin_amdgcn_raw_buffer_load_b128(s,base+lane*16,0,16); r.b=__builtin_amdgcn_raw_buffer_load_b128(s,base+1024+lane*16,0,16);
  r.c=__builtin_amdgcn_raw_buffer_load_b64(s,base+2048+lane*8,0,16); return r;
}
DI void add_quarter(float (&t)[20], const Qd& r){
  #pragma unroll
  for(int k=0;k<4;k++){ t[2*k]+=bflo(r.a[k]); t[2*k+1]+=bfhi(r.a[k]); t[8+2*k]+=bflo(r.b[k]); t[9+2*k]+=bfhi(r.b[k]); }
  #pragma unroll
  for(int k=0;k<2;k++){ t[16+2*k]+=bflo(r.c[k]); t[17+2*k]+=bfhi(r.c[k]); }
}

DI void p2_mfma(const Prm& p, LAS char* lds, int q, int pm, int pn, unsigned w, unsigned lane, u64 tag TSP){
  const int tile=pm*4+pn;
  f32x4 acc[4][5];
  const srd_t sY=mksrd(p.ws+WS_YX), sX=mksrd(p.ws+WS_FX);
  const unsigned wr=w>>1, wc=w&1, g=lane>>4, c16=lane&15;
  const unsigned trow=pm*128+wr*64+q*16+c16, ecol=pn*160+wc*80+4*g;
  const unsigned sbase=((tile*4+q)*16+w)*2560, rbase=((tile*16+q)*4+w)*2560;
  const unsigned pY=WS_YF+((tile*4+w)*4+(lane&3))*8, pO=WS_OF+((tile*4+w)*4+(lane&3))*8;
  u64 pv=0;
  { const unsigned pH=WS_HF+(pm*16+q*4+(lane&3))*8;
    p2_loop<false,0,NY>(lds,q,w,lane,acc,[&](auto T,auto M){ constexpr int t=T.value, m=M.value;
      if constexpr(t==T_PH && m==17){ if(w==1) poll_issue(pv,p.ws,pH); }
      if constexpr(t==NY-4 && m==-1){ if(w==1){ wait_vm<0>(); poll_check(pv,p.ws,pH,tag); } } }); }
  TS(7);
  Qd yq[3], y0=pack_quarter(acc[0]);
  #pragma unroll
  for(int k=0;k<3;k++) yq[k]=pack_quarter(acc[k+1]);
  asm volatile("":"+v"(y0.a),"+v"(y0.b),"+v"(y0.c));
  GAP();
  p2_loop<true,NY,NO>(lds,q,w,lane,acc,[&](auto T,auto M){ constexpr int t=T.value, m=M.value;
    if constexpr(t<3 && (m==14||m==16||m==18)) put_part<(m-14)/2>(sY,sbase+((q+1+t)&3)*4*2560,lane,yq[t<3? t: 0]);
    if constexpr(t==4 && m==18){ wait_vm<0>(); if(lane==0) st_flag(p.ws,WS_YF+((tile*4+w)*4+q)*8,tag); }
    if constexpr(t==5 && m==38) poll_issue(pv,p.ws,pY); });
  TS(8);
  float to[20];
  #pragma unroll
  for(int k=1;k<4;k++){ const Qd v=pack_quarter(acc[k]); const unsigned b=sbase+((q+k)&3)*4*2560;
    put_part<0>(sX,b,lane,v); put_part<1>(sX,b,lane,v); put_part<2>(sX,b,lane,v); }
  #pragma unroll
  for(int k=0;k<20;k++) to[k]=acc[0][k>>2][k&3];
  TS(9);
  wait_vm<9>(); poll_check(pv,p.ws,pY,tag);
  { Qd y[3], r[3]; u32x2 xv[5];
    #pragma unroll
    for(int k=0;k<3;k++) y[k]=get_quarter(sY,rbase+((q+1+k)&3)*16*2560,lane);
    #pragma unroll
    for(int fn=0;fn<5;fn++) xv[fn]=*(const u32x2*)(p.x+(size_t)trow*D_+ecol+16*fn);
    GAP();
    wait_vm<14>(); if(lane==0) st_flag(p.ws,WS_OF+((tile*4+w)*4+q)*8,tag);
    TS(12);
    wait_flags(p.ws,pO,tag);
    TS(13);
    #pragma unroll
    for(int k=0;k<3;k++) r[k]=get_quarter(sX,rbase+((q+1+k)&3)*16*2560,lane);
    TS(10);
    float ty[20];
    #pragma unroll
    for(int k=0;k<20;k++) ty[k]=0.f;
    add_quarter(ty,y0);
    #pragma unroll
    for(int k=0;k<3;k++){ add_quarter(ty,y[k]); add_quarter(to,r[k]); }
    #pragma unroll
    for(int fn=0;fn<5;fn++){ u32x2 o;
      #pragma unroll
      for(int k=0;k<2;k++){ const float o0=to[4*fn+2*k], o1=to[4*fn+2*k+1];
        o[k]=pk(bflo(xv[fn][k])+ty[4*fn+2*k]*(o0*sigm(o0)), bfhi(xv[fn][k])+ty[4*fn+2*k+1]*(o1*sigm(o1))); }
      *(u32x2*)(p.out+(size_t)trow*D_+ecol+16*fn)=o; }
  }
  TS(11);
}
__global__ __launch_bounds__(512,2) void fnet_mlp_fused(Prm p){
  extern __shared__ __attribute__((aligned(1024))) char smem[];
  LAS char* lds=(LAS char*)smem;
  const unsigned tid=threadIdx.x, lane=tid&63, w=rfl(tid>>6);
  const int b=blockIdx.x, x8=b&7, r=b>>3;
  const int q=x8>>1, pm=(x8&1)*8+(r>>2), pn=r&3, j=q*4+pn;
  TSR(14); TS(0);
  if(w>=4){
    const unsigned lw=w-4;
    const u64 tag= lw<2? loader<3>(p,lds,pm,j,q,pn,lw,lane TSA) : loader<2>(p,lds,pm,j,q,pn,lw,lane TSA);
    TS(8);
    if(b==0 && lw==0){
      #pragma unroll 1
      for(int i=0;i<4;i++) wait_flags(p.ws,WS_ABF+(i*64+lane)*8,tag);
      if(lane==0) st_flag(p.ws,0,tag);
    }
  } else {
    const u64 tag=p1_mfma(p,lds,pm,j,w,lane TSA);
    p2_mfma(p,lds,q,pm,pn,w,lane,tag TSA);
  }
  TSR(15); TSPUT();
}

extern "C" void kernel_launch(void* const* d_in, const int* in_sizes, int n_in, void* d_out, int out_size, void* d_ws, size_t ws_size, hipStream_t stream) {
  Prm p; p.x=(const u16*)d_in[0]; p.wsre=(const u16*)d_in[1]; p.wsim=(const u16*)d_in[2]; p.wdre=(const u16*)d_in[3]; p.wdim=(const u16*)d_in[4];
  p.w1=(const u16*)d_in[6]; p.w2=(const u16*)d_in[7]; p.out=(u16*)d_out; p.ws=(char*)d_ws;
  (void)hipFuncSetAttribute((const void*)fnet_mlp_fused, hipFuncAttributeMaxDynamicSharedMemorySize, LDS_BYTES);
  fnet_mlp_fused<<<256,512,LDS_BYTES,stream>>>(p);
}
```

```cpp
#define PADT 2800
#include <hip/hip_runtime.h>
#include <stdint.h>
#include <type_traits>
#include <utility>
typedef unsigned short u16;
typedef unsigned long long u64;
typedef float f32x4 __attribute__((ext_vector_type(4)));
typedef float f32x2 __attribute__((ext_vector_type(2)));
typedef __bf16 bf16x8 __attribute__((ext_vector_type(8)));
typedef __bf16 bf16x2 __attribute__((ext_vector_type(2)));
typedef short s16x4 __attribute__((ext_vector_type(4)));
typedef short s16x8 __attribute__((ext_vector_type(8)));
typedef unsigned u32x4 __attribute__((ext_vector_type(4)));
typedef unsigned u32x2 __attribute__((ext_vector_type(2)));
#define DI __device__ __forceinline__
#define GAP() __builtin_amdgcn_sched_barrier(0)
#define LAS __attribute__((address_space(3)))
#define GAS __attribute__((address_space(1)))
typedef GAS u64 gu64;
typedef GAS unsigned gu32;
typedef __amdgpu_buffer_rsrc_t srd_t;

constexpr int S_=2048, D_=640, F_=1536;
constexpr unsigned WS_ABF=8192, WS_HF=12288, WS_YF=16384, WS_OF=24576, WS_RST=65536;
constexpr unsigned WS_ABT=1u<<20, WS_H=8u<<20, WS_YX=16u<<20, WS_FX=28u<<20;
constexpr int STG1=28672, O_W=16384, ABS=4*STG1, AUX=155648, ROWM=AUX, ROWR=AUX+512, CSA=AUX+1024, CSH=AUX+2048, STG=AUX+4096, LDS_BYTES=163840;
constexpr int slot1(int i){ return (i<10? i%5: (i-10)%4)*STG1; }
constexpr int PA[4]={114688,131072,98304,20480}, PB[4]={57344,77824,0,36864}, HSTG=20480;
constexpr unsigned SPIN_MAX=40000;
#define ROTEXPR ((pn+pm)&3)
#define K0EXPR (5*((pn+pm)&1))
constexpr int NY=16, NO=6, NT=NY+NO;

#define TS(i)
#define TT(ph,t)
#define TSR(l)
#define TSPUT()
#define TSP
#define TSA

template<int I> using ic=std::integral_constant<int,I>;
template<int... I,class F> DI void sfor_(std::integer_sequence<int,I...>, F&& f){ (f(ic<I>{}),...); }
template<int N,class F> DI void sfor(F&& f){ sfor_(std::make_integer_sequence<int,N>{},f); }
DI unsigned rfl(unsigned x){ return __builtin_amdgcn_readfirstlane(x); }
DI float bflo(unsigned v){ return __uint_as_float(v<<16); }
DI float bfhi(unsigned v){ return __uint_as_float(v&0xffff0000u); }
DI unsigned pk(float lo,float hi){ f32x2 v={lo,hi}; bf16x2 b=__builtin_convertvector(v,bf16x2); return __builtin_bit_cast(unsigned,b); }
DI float sigm(float v){ return __builtin_amdgcn_rcpf(1.f+__builtin_amdgcn_exp2f(-1.4426950408889634f*v)); }
template<int N> DI void wait_vm(){ asm volatile("s_waitcnt vmcnt(%0)"::"n"(N):"memory"); }
DI void bar(){ __builtin_amdgcn_s_barrier(); }
DI void wait_lgkm(){ __builtin_amdgcn_s_waitcnt(0xC07F); }
constexpr unsigned BIAS=4096;
#define DMA1(O,X) "buffer_load_dwordx4 %" #O ", %1, %2 offen" X " lds"
#define DMA_GROUP(X) \
  if constexpr(N==1) asm volatile("s_mov_b32 m0, %0\n\ts_nop 0\n\tbuffer_load_dwordx4 %3, %1, %2 offen" X " lds" :: "s"(m0v),"s"(srd),"s"(soff),"v"(v[0]) : "memory"); \
  if constexpr(N==2) asm volatile("s_mov_b32 m0, %0\n\ts_nop 0\n\tbuffer_load_dwordx4 %3, %1, %2 offen" X " lds\n\tbuffer_load_dwordx4 %4, %1, %2 offen offset:1024" X " lds" :: "s"(m0v),"s"(srd),"s"(soff),"v"(v[0]),"v"(v[1]) : "memory"); \
  if constexpr(N==3) asm volatile("s_mov_b32 m0, %0\n\ts_nop 0\n\tbuffer_load_dwordx4 %3, %1, %2 offen" X " lds\n\tbuffer_load_dwordx4 %4, %1, %2 offen offset:1024" X " lds\n\tbuffer_load_dwordx4 %5, %1, %2 offen offset:2048" X " lds" :: "s"(m0v),"s"(srd),"s"(soff),"v"(v[0]),"v"(v[1]),"v"(v[2]) : "memory"); \
  if constexpr(N==4) asm volatile("s_mov_b32 m0, %0\n\ts_nop 0\n\tbuffer_load_dwordx4 %3, %1, %2 offen" X " lds\n\tbuffer_load_dwordx4 %4, %1, %2 offen offset:1024" X " lds\n\tbuffer_load_dwordx4 %5, %1, %2 offen offset:2048" X " lds\n\tbuffer_load_dwordx4 %6, %1, %2 offen offset:3072" X " lds" :: "s"(m0v),"s"(srd),"s"(soff),"v"(v[0]),"v"(v[1]),"v"(v[2]),"v"(v[3]) : "memory");
#define SC1S " sc1"
template<int N,bool DEV> DI void dma_group(const __amdgpu_buffer_rsrc_t& srd, const unsigned* v, unsigned soff, unsigned m0v){
  if constexpr(DEV){ DMA_GROUP(SC1S) } else { DMA_GROUP("") }
}
template<bool DEV> DI void dma5(const __amdgpu_buffer_rsrc_t& srd, const unsigned* v, unsigned soff, unsigned m0v){
  dma_group<4,DEV>(srd,v,soff,m0v); dma_group<1,DEV>(srd,v+4,soff,m0v+4096u);
}
DI srd_t mksrd(const void* p){ return __builtin_amdgcn_make_buffer_rsrc((void*)p,0,0x7ffffff0,0x00020000); }
DI srd_t mksrd_lo(const void* p){ return mksrd((const char*)p-BIAS); }
DI void st_flag(char* ws, unsigned off, u64 tag){ __hip_atomic_store((gu64*)(ws+off), tag, __ATOMIC_RELAXED, __HIP_MEMORY_SCOPE_AGENT); }
DI void st_f32(char* ws, unsigned off, float v){ __hip_atomic_store((gu32*)(ws+off), __float_as_uint(v), __ATOMIC_RELAXED, __HIP_MEMORY_SCOPE_AGENT); }
DI u64 ld_flag(char* ws, unsigned off){ return __hip_atomic_load((gu64*)(ws+off), __ATOMIC_RELAXED, __HIP_MEMORY_SCOPE_AGENT); }
DI void wait_flags(char* ws, unsigned off, u64 tag){
  #pragma unroll 1
  for(unsigned s=0;s<SPIN_MAX;s++){ const u64 v=ld_flag(ws,off); if(__all(v==tag)) break; __builtin_amdgcn_s_sleep(2); }
}
DI u64 get_tag(const char* ws){ u64 ep; asm volatile("s_load_dwordx2 %0, %1, 0x0\n\ts_waitcnt lgkmcnt(0)":"=s"(ep):"s"(ws):"memory"); return ep+1; }
DI void poll_issue(u64& v, char* ws, unsigned off){ const char* a=ws+off; asm volatile("global_load_dwordx2 %0, %1, off sc1":"=v"(v):"v"(a):"memory"); }
DI void poll_check(u64& v, char* ws, unsigned off, u64 tag){ asm volatile("":"+v"(v)); if(!__all(v==tag)) wait_flags(ws,off,tag); }
DI bf16x8 ldf(const LAS char* a){ return *(const LAS bf16x8*)a; }
DI bf16x8 ldtr(const LAS char* a, int step){
  const s16x4 lo=__builtin_amdgcn_ds_read_tr16_b64_v4i16((LAS s16x4*)a), hi=__builtin_amdgcn_ds_read_tr16_b64_v4i16((LAS s16x4*)(a+step));
  return __builtin_bit_cast(bf16x8,(s16x8)__builtin_shufflevector(lo,hi,0,1,2,3,4,5,6,7));
}
DI s16x4 ldtr1(const LAS char* a){ return __builtin_amdgcn_ds_read_tr16_b64_v4i16((LAS s16x4*)a); }
DI bf16x8 cat(s16x4 lo, s16x4 hi){ return __builtin_bit_cast(bf16x8,(s16x8)__builtin_shufflevector(lo,hi,0,1,2,3,4,5,6,7)); }
DI f32x4 mma(bf16x8 a, bf16x8 b, f32x4 c){ return __builtin_amdgcn_mfma_f32_16x16x32_bf16(a,b,c,0,0,0); }

template<int CTRL> DI float dpp_add(float v){ return v+__int_as_float(__builtin_amdgcn_update_dpp(0,__float_as_int(v),CTRL,0xf,0xf,true)); }
DI float sum16(float v){ v=dpp_add<0xB1>(v); v=dpp_add<0x4E>(v); v=dpp_add<0x141>(v); return dpp_add<0x140>(v); }

struct Prm { const u16* x; const u16* wsre; const u16* wsim; const u16* wdre; const u16* wdim; const u16* w1; const u16* w2; u16* out; char* ws; };

enum { K_P1, K_YA, K_YB, K_O, K_STAT, K_STCHK, K_ABS, K_FAB, K_PAB, K_CAB, K_HRD, K_HS, K_FH };
struct Op { int k, i, step; };
struct Sched { Op op[160]; int n; };
constexpr int TB1=20, TP2=21, TEND=TP2+NT;
#define ST_FAB 14
#define ST_FH 8
#define T_PH 10
constexpr Sched make_sched(){
  Sched s{}; auto add=[&](int k,int i,int st){ s.op[s.n].k=k; s.op[s.n].i=i; s.op[s.n].step=st; s.n++; };
  add(K_P1,0,-2); add(K_P1,1,-2); add(K_P1,2,-1); add(K_P1,3,-1); add(K_P1,4,-1);
  for(int st=0;st<20;st++){
    if(st==4) add(K_STAT,0,st);
    if(st==ST_FAB) add(K_FAB,0,st);
    if(st<=8) add(K_P1,st+5,st); else if(st>=10 && st<=15) add(K_P1,st+4,st);
    if(st==8) add(K_STCHK,0,st);
    if(st==10) add(K_ABS,0,st);
    if(st==15) add(K_YA,0,st);
    if(st==16) add(K_YA,1,st);
    if(st==18) add(K_PAB,0,st);
    if(st==19){ add(K_CAB,0,st); add(K_YB,0,st); add(K_YB,1,st); add(K_YA,2,st); add(K_YB,2,st); }
  }
  add(K_HRD,0,TB1);
  for(int t=0;t<NT;t++){ const int st=TP2+t;
    if(t==ST_FH) add(K_FH,0,st);
    if(t==0){ add(K_YA,3,st); add(K_YB,3,st); }
    if(t<6) add(K_HS,t,st);
    if(t+4<NY){ add(K_YA,t+4,st); add(K_YB,t+4,st); } else if(t+4<NT) add(K_O,t+4,st);
  }
  return s;
}
constexpr Sched SCH=make_sched();
constexpr int op_cnt(const Op& o,int nwd){ return o.k==K_P1? (o.i<10? 4+nwd: 7): o.k==K_YA? 4: o.k==K_YB? 5: o.k==K_O? 9: o.k==K_ABS? 5: (o.k==K_HS||o.k==K_PAB)? 1: 0; }
constexpr bool needed(const Op& o,int tau){
  if(tau<19 && o.k==K_P1 && o.i==tau+1) return true;
  if(tau==8 && o.k==K_STAT) return true;
  if(tau>=TB1 && tau<TEND-1){ const int t=tau-TB1; if((o.k==K_YA||o.k==K_YB||o.k==K_O) && o.i==t) return true; }
  return false; }
constexpr int last_needed(int tau){ int l=-1; for(int n=0;n<SCH.n;n++) if(SCH.op[n].step<tau && needed(SCH.op[n],tau)) l=n; return l; }
constexpr int allowed(int tau,int nwd){ const int l=last_needed(tau); if(l<0) return -1; int c=0; for(int n=l+1;n<SCH.n;n++) if(SCH.op[n].step<tau) c+=op_cnt(SCH.op[n],nwd); return c; }
constexpr int find_op(int k,int i){ for(int n=0;n<SCH.n;n++) if(SCH.op[n].k==k && SCH.op[n].i==i) return n; return 1<<20; }
constexpr bool done_by(int k,int i,int tau){ const int p=find_op(k,i); for(int t=-1;t<=tau;t++) if(last_needed(t)>=p) return true; return false; }
constexpr int step_begin(int st){ int n=0; while(n<SCH.n && SCH.op[n].step<st) n++; return n; }
static_assert(done_by(K_ABS,0,ST_FAB) && done_by(K_HS,5,TP2+ST_FH),"a flag follows its completed stores");
static_assert(T_PH<NY-4,"schedule");

template<int NWD>
DI u64 loader(const Prm& p, LAS char* lds, int pm, int j, int q, int pn, unsigned lw, unsigned lane TSP){
  const unsigned ldsb=(unsigned)(size_t)lds;
  const unsigned r=lane>>3;
  const int rot=ROTEXPR, k0=K0EXPR;
  u64 tag, pv=0;
  const srd_t sA=mksrd_lo(p.x+(size_t)pm*128*D_), sD=mksrd_lo((j<8? p.wdre: p.wdim)+(size_t)(j&7)*80*D_), sW=mksrd_lo(p.w1+j*96);
  const unsigned q0= NWD==3? 3*lw: 2*lw+2;
  unsigned vX[4], vD[NWD], vW[3];
  #pragma unroll
  for(int i=0;i<4;i++) vX[i]=(32*lw+16*(i&1)+4*(i>>1)+8*(r>>2)+(r&3))*(D_*2)+((lane&7)^((i&1)*4+(r>>1)))*16+BIAS-1024*i;
  #pragma unroll
  for(int i=0;i<NWD;i++) vD[i]=((q0+i)*8+r)*(D_*2)+((lane&7)^(((q0+i)&1)*4+(r>>1)))*16+BIAS-1024*i;
  #pragma unroll
  for(int i=0;i<3;i++){ const unsigned c=i*64+lane, row=c/12, ch=c%12; vW[i]=(16*lw+row)*(F_*2)+32*((ch>>1)^((row>>3)&1))+16*(ch&1)+BIAS-1024*i; }
  auto p1=[&](auto I){ constexpr int i=I.value; int kt=k0+(i%10); kt= kt>=10? kt-10: kt;
    dma_group<4,false>(sA,vX,kt*128u,ldsb+lw*4096u+slot1(i));
    if constexpr(i<10) dma_group<NWD,false>(sD,vD,kt*128u,ldsb+O_W+q0*1024u+slot1(i));
    else dma_group<3,false>(sW,vW,kt*(64u*F_*2),ldsb+O_W+lw*3072u+slot1(i)); };
  TS(1);
  p1(ic<0>{}); p1(ic<1>{});
  TS(2);
  tag=get_tag(p.ws);
  const srd_t sWs=mksrd_lo((q<2? p.wsre: p.wsim)+(size_t)pm*128*S_+(q&1)*1024), sT=mksrd_lo(p.ws+WS_ABT+(size_t)pn*160*8192+q*2048);
  const srd_t sH=mksrd_lo(p.ws+WS_H+((size_t)pm*48+q*12)*8192), sW2=mksrd_lo(p.w2+(size_t)q*384*D_+pn*160), sWk=mksrd(p.ws);
  unsigned vA[4], vB[5], vH[4], vW2[5];
  #pragma unroll
  for(int i=0;i<4;i++){ const unsigned c=(lane&7)^((i&1)*4+(r>>1));
    vA[i]=(32*lw+8*i+r)*(S_*2)+c*16+BIAS-1024*i; vH[i]=(c>>2)*8192+(32*lw+8*i+r)*64+(c&3)*16+BIAS-1024*i; }
  #pragma unroll
  for(int i=0;i<5;i++){ const unsigned bi=BIAS-(i<4? 1024*i: 0);
    vB[i]=((5*lw+i)*8+r)*8192+((lane&7)^(((lw+i)&1)*4+(r>>1)))*16+bi;
    const unsigned qq=i*64+lane, row=qq/20, ch=qq%20; vW2[i]=(16*lw+row)*(D_*2)+32*((ch>>1)^((row>>3)&1))+16*(ch&1)+bi; }
  const unsigned lA2=ldsb+lw*4096u, lB2=ldsb+lw*5120u;
  u32x4 hv[6];
  const unsigned pAB=WS_ABF+((((q>>1)*4+pn)*16+(q&1)*8)*2+(lane&15))*8;
  auto exec=[&](auto N){ constexpr Op o=SCH.op[N.value];
    if constexpr(o.k==K_P1) p1(ic<o.i>{});
    if constexpr(o.k==K_YA){ const int u=(o.i&~3)|((o.i+rot)&3); dma_group<4,false>(sWs,vA,u*128u,lA2+PA[o.i&3]); }
    if constexpr(o.k==K_YB){ const int u=(o.i&~3)|((o.i+rot)&3); dma5<true>(sT,vB,u*128u,lB2+PB[o.i&3]); }
    if constexpr(o.k==K_O){ int ko=o.i-NY+rot; ko= ko>=NO? ko-NO: ko;
      dma_group<4,true>(sH,vH,ko*16384u,lA2+PA[o.i&3]); dma5<false>(sW2,vW2,ko*(64u*D_*2),lB2+PB[o.i&3]); }
    if constexpr(o.k==K_STAT){ if(NWD==3 && lw==0){ const unsigned v0=WS_RST+pm*2048+lane*16, vs[2]={v0,v0}; dma_group<2,true>(sWk,vs,0,ldsb+STG); } }
    if constexpr(o.k==K_STCHK){ if(NWD==3 && lw==0){
        u32x4 a=*(const LAS u32x4*)(lds+STG+lane*16), b=*(const LAS u32x4*)(lds+STG+1024+lane*16);
        const unsigned tg=(unsigned)tag;
        if(!__all(a[1]==tg && a[3]==tg && b[1]==tg && b[3]==tg)){
          #pragma unroll 1
          for(unsigned sp=0;sp<SPIN_MAX;sp++){ asm volatile("":::"memory");
            a=__builtin_amdgcn_raw_buffer_load_b128(sWk,WS_RST+pm*2048+lane*16,0,16); b=__builtin_amdgcn_raw_buffer_load_b128(sWk,WS_RST+pm*2048+1024+lane*16,0,16);
            if(__all(a[1]==tg && a[3]==tg && b[1]==tg && b[3]==tg)) break; } }
        LAS unsigned* rm=(LAS unsigned*)(lds+ROWM); LAS unsigned* rr=(LAS unsigned*)(lds+ROWR);
        rm[lane]=a[0]; rr[lane]=a[2]; rm[64+lane]=b[0]; rr[64+lane]=b[2];
        wait_lgkm(); } }
    if constexpr(o.k==K_ABS){
      u32x4 v[5];
      #pragma unroll
      for(int n=0;n<5;n++){ const unsigned col=20*lw+4*n+(lane>>4); v[n]=*(const LAS u32x4*)(lds+ABS+col*256+(((lane&15)^(col&15))*16)); }
      #pragma unroll
      for(int n=0;n<5;n++){ const unsigned col=20*lw+4*n+(lane>>4);
        __builtin_amdgcn_raw_buffer_store_b128(v[n],sWk,WS_ABT+((j&7)*80+col)*8192+(j>>3)*4096+pm*256+(lane&15)*16,0,16); } }
    if constexpr(o.k==K_FAB){ if(lw==0 && lane==0) st_flag(p.ws,WS_ABF+((((j>>3)*4+((j&7)>>1))*16+pm)*2+(j&1))*8,tag); }
    if constexpr(o.k==K_PAB) poll_issue(pv,p.ws,pAB);
    if constexpr(o.k==K_CAB){ wait_vm<0>(); poll_check(pv,p.ws,pAB,tag); }
    if constexpr(o.k==K_HRD){
      #pragma unroll
      for(int rg=0;rg<2;rg++){ const unsigned s=(lw*2+rg)*16+(lane>>2), sw=((s&3)<<2)^(((s>>1)&1)|(((s>>3)&1)<<1));
        #pragma unroll
        for(int G=0;G<3;G++) hv[rg*3+G]=*(const LAS u32x4*)(lds+HSTG+s*256+(((4*G+(lane&3))^sw)*16)); }
      wait_lgkm(); }
    if constexpr(o.k==K_HS) __builtin_amdgcn_raw_buffer_store_b128(hv[o.i],sWk,WS_H+(pm*48+3*j+o.i%3)*8192+((lw*2+o.i/3)*16+(lane>>2))*64+(lane&3)*16,0,16);
    if constexpr(o.k==K_FH){ if(lw==0 && lane==0) st_flag(p.ws,WS_HF+(pm*16+j)*8,tag); }
  };
  sfor<TEND+1>([&](auto I){ constexpr int tau=I.value-1, al=allowed(tau,NWD);
    static_assert(al<64,"vmcnt range");
    if constexpr(al>=0) wait_vm<al>();
    if constexpr(tau==-1) TS(3);
    bar();
    if constexpr(tau==-1) TS(5); else if constexpr(tau<TB1) TT(0,tau); else if constexpr(tau==TB1) TS(4); else TT(1,tau-TP2);
    constexpr int b0=step_begin(tau), b1=step_begin(tau+1);
    sfor<b1-b0>([&](auto M){ exec(ic<b0+M.value>{}); });
  });
  return tag;
}
DI u64 p1_mfma(const Prm& p, LAS char* lds, int pm, int j, unsigned w, unsigned lane TSP){
  u64 tag;
  const unsigned wr=w>>1, wc=w&1, g=lane>>4, c16=lane&15;
  if(w<2){ const unsigned row=pm*128+j*8+w*4+g; u32x4 v[5]; float s=0;
    #pragma unroll
    for(int i=0;i<5;i++) v[i]=*(const u32x4*)(p.x+(size_t)row*D_+(c16+16*i)*8);
    tag=get_tag(p.ws);
    #pragma unroll
    for(int i=0;i<5;i++)
      #pragma unroll
      for(int k=0;k<4;k++) s+=bflo(v[i][k])+bfhi(v[i][k]);
    const float m=sum16(s)*(1.f/D_); float q2=0;
    #pragma unroll
    for(int i=0;i<5;i++)
      #pragma unroll
      for(int k=0;k<4;k++){ const float a=bflo(v[i][k])-m, c=bfhi(v[i][k])-m; q2+=a*a+c*c; }
    const float rs=1.0f/sqrtf(sum16(q2)*(1.f/D_)+1e-5f);
    if(c16<2) st_flag(p.ws,WS_RST+row*16+c16*8, ((u64)(unsigned)tag<<32)|__float_as_uint(c16==0? m: rs));
  } else tag=get_tag(p.ws);
  TS(1);
  const u32x4 one4={0x3F803F80u,0x3F803F80u,0x3F803F80u,0x3F803F80u}; const bf16x8 ones=__builtin_bit_cast(bf16x8,one4);
  const unsigned rho=3*wr;
  unsigned aA[2], aB[2][5], hA[2], hB[2][3];
  #pragma unroll
  for(int s=0;s<2;s++){
    { const unsigned ch=((4*(s^wc)+g)^((lane>>1)&7))*16; aA[s]=(32*w+c16)*128+ch;
      #pragma unroll
      for(int f=0;f<5;f++){ const unsigned fr=(f+rho)%5; aB[s][f]=O_W+(fr*16+c16)*128+ch; } }
    { const unsigned ks=s^wr, ch=((4*ks+g)^((lane>>1)&7))*16; hA[s]=(wr*64+c16)*128+ch;
      #pragma unroll
      for(int f=0;f<3;f++) hB[s][f]=O_W+192*(8*g+(c16>>2))+8*(lane&3)+32*((3*wc+f)^(g&1))+ks*6144; }
  }
  f32x4 acc[2][5], csa[3];
  #pragma unroll
  for(int k=0;k<5;k++) acc[0][k]=acc[1][k]=(f32x4){0.f,0.f,0.f,0.f};
  csa[0]=csa[1]=csa[2]=(f32x4){0.f,0.f,0.f,0.f};
  bf16x8 fa[2][2], fb[2][5], ga[2][4], gb[2][3]; s16x4 tl[3];
  auto rdA=[&](int m,const LAS char* base,int s){
    if(m==0) fa[s][0]=ldf(base+aA[s]); else if(m<=5) fb[s][m-1]=ldf(base+aB[s][m-1]); else fa[s][1]=ldf(base+aA[s]+2048); };
  auto rdH=[&](int m,const LAS char* base,int s){
    if(m==0) ga[s][0]=ldf(base+hA[s]);
    else if(m<=6){ const int c=(m-1)/2; if(m&1) tl[c]=ldtr1(base+hB[s][c]); else gb[s][c]=cat(tl[c],ldtr1(base+hB[s][c]+768)); }
    else ga[s][m-6]=ldf(base+hA[s]+(m-6)*2048); };
  bar();
  TS(2);
  #pragma unroll
  for(int m=0;m<7;m++) rdA(m,lds,0);
  GAP();
  const unsigned s0=32*w+8*g;
  f32x4 m0,m1,r0,r1; float c[5];
  sfor<10>([&](auto T){ constexpr int t=T.value;
    #pragma unroll
    for(int s=0;s<2;s++){
      const bool last=(s==1);
      if(last && t==9){ LAS float* cw=(LAS float*)(lds+CSA)+wc*80;
        if(g==0){ cw[rho*16+c16]=csa[0][0]; cw[(rho+1)*16+c16]=csa[1][0]; if(wr==0) cw[32+c16]=csa[2][0]; } }
      wait_lgkm();
      if(last) bar();
      GAP();
      const LAS char* base=lds+slot1(last? t+1: t);
      if(last && t==9){ const LAS float* cs=(const LAS float*)(lds+CSA);
        #pragma unroll
        for(int f=0;f<5;f++){ const unsigned fr=(f+rho)%5; c[f]=cs[fr*16+c16]+cs[80+fr*16+c16]; }
        m0=*(const LAS f32x4*)(lds+ROWM+s0*4); m1=*(const LAS f32x4*)(lds+ROWM+s0*4+16); r0=*(const LAS f32x4*)(lds+ROWR+s0*4); r1=*(const LAS f32x4*)(lds+ROWR+s0*4+16); }
      constexpr int NM= 13;
      #pragma unroll
      for(int m=0;m<NM;m++){
        if(!last){ if(m<3) csa[m]=mma(ones,fb[s][m],csa[m]); else { const int mm=m-3, fm=mm/5, f0=mm%5, fn=fm?4-f0:f0; acc[fm][fn]=mma(fa[s][fm],fb[s][fn],acc[fm][fn]); } }
        else if(m<10){ const int fm=m/5, f0=m%5, fn=fm?4-f0:f0; acc[fm][fn]=mma(fa[s][fm],fb[s][fn],acc[fm][fn]); }
        if(last && t==9){ if(m<10) rdH(m,base,0); }
        else if(m<7) rdA(m,base,s^1);
        GAP();
      }
    }
  });
  TS(3);
  { const float sg= j<8? 1.f: -1.f;
    r0*=sg; r1*=sg;
    #pragma unroll
    for(int f=0;f<5;f++){ const unsigned col=((f+rho)%5)*16+c16;
      const f32x4 a=(acc[0][f]-m0*c[f])*r0, b=(acc[1][f]-m1*c[f])*r1;
      const u32x4 o={pk(a[0],a[1]),pk(a[2],a[3]),pk(b[0],b[1]),pk(b[2],b[3])};
      *(LAS u32x4*)(lds+ABS+col*256+(((4*w+g)^c16)*16))=o; }
  }
  GAP();
  TS(4);
  f32x4 hac[4][3], csh[3], c4[3];
  #pragma unroll
  for(int k=0;k<3;k++){ csh[k]=(f32x4){0.f,0.f,0.f,0.f};
    #pragma unroll
    for(int i=0;i<4;i++) hac[i][k]=(f32x4){0.f,0.f,0.f,0.f}; }
  float hm[4], hr[4]; unsigned hb[4], hx[4];
  sfor<10>([&](auto T){ constexpr int t=T.value, i=10+t;
    #pragma unroll
    for(int s=0;s<2;s++){
      const bool last=(s==1);
      if(last && t==9){ LAS float* cw=(LAS float*)(lds+CSH)+wr*96+wc*48;
        if(c16==0){
          #pragma unroll
          for(int f=0;f<3;f++) *(LAS f32x4*)(cw+16*f+4*g)=csh[f]; } }
      wait_lgkm();
      if(last) bar();
      GAP();
      const LAS char* base=lds+slot1(last? (t==9? i: i+1): i);
      if(last && t==9){ const LAS float* cs=(const LAS float*)(lds+CSH)+wc*48;
        #pragma unroll
        for(int f=0;f<3;f++) c4[f]=*(const LAS f32x4*)(cs+16*f+4*g)+*(const LAS f32x4*)(cs+96+16*f+4*g);
        #pragma unroll
        for(int fm=0;fm<4;fm++){ const unsigned s=wr*64+32*(fm>>1)+16*(c16>>3)+8*((c16>>2)&1)+4*(fm&1)+(c16&3);
          hm[fm]=((const LAS float*)(lds+ROWM))[s]; hr[fm]=((const LAS float*)(lds+ROWR))[s];
          hb[fm]=HSTG+s*256+(g&1)*8; hx[fm]=((g>>1)^((s&3)<<2)^(((s>>1)&1)|(((s>>3)&1)<<1)))*16; } }
      #pragma unroll
      for(int m=0;m<15;m++){
        if(!last){ if(m<3) csh[m]=mma(gb[s][m],ones,csh[m]); else { const int mm=m-3, fm=mm/3, f0=mm%3, fn=(fm&1)?2-f0:f0; hac[fm][fn]=mma(gb[s][fn],ga[s][fm],hac[fm][fn]); } }
        else if(m<12){ const int fm=m/3, f0=m%3, fn=(fm&1)?2-f0:f0; hac[fm][fn]=mma(gb[s][fn],ga[s][fm],hac[fm][fn]); }
        if(m<10 && !(last && t==9)) rdH(m,base,s^1);
        GAP();
      }
    }
  });
  TS(5);
  #pragma unroll
  for(int fm=0;fm<4;fm++){ const float zr=hr[fm]*-1.4426950408889634f;
    #pragma unroll
    for(int f=0;f<3;f++){ const f32x4 v=hac[fm][f]-c4[f]*hm[fm], a=v*hr[fm], z=v*zr; f32x4 e;
      #pragma unroll
      for(int k=0;k<4;k++) e[k]=__builtin_amdgcn_exp2f(z[k]);
      e+=1.f;
      #pragma unroll
      for(int k=0;k<4;k++) e[k]=__builtin_amdgcn_rcpf(e[k]);
      const f32x4 h=a*e; const u32x2 o={pk(h[0],h[1]),pk(h[2],h[3])};
      *(LAS u32x2*)(lds+hb[fm]+(hx[fm]^((3*wc+f)*32)))=o; } }
  wait_lgkm();
  bar();
  TS(6);
  return tag;
}

template<bool TR,int T0,int N,class Hook>
DI void p2_loop(LAS char* lds, int q, unsigned w, unsigned lane, f32x4 (&acc)[4][5], Hook hook){
  const unsigned wr=w>>1, wc=w&1, g=lane>>4, c16=lane&15;
  unsigned adA[2][4], adB[2];
  #pragma unroll
  for(int s=0;s<2;s++){ const unsigned ch=((4*s+g)^((lane>>1)&7))*16;
    #pragma unroll
    for(int fm=0;fm<4;fm++) adA[s][fm]=(wr*64+((fm+q)&3)*16+c16)*128+ch;
    if(!TR) adB[s]=(wc*80+c16)*128+ch;
    else { const int sgn=((wc+s)&1)? -32: 32; adB[s]=320*(8*g+(c16>>2))+8*(lane&3)+160*wc+sgn*(int)(g&1); } }
  #pragma unroll
  for(int i=0;i<4;i++)
    #pragma unroll
    for(int k=0;k<5;k++) acc[i][k]=(f32x4){0.f,0.f,0.f,0.f};
  bf16x8 fa[2][4], fb[2][5]; s16x4 tl[5];
  constexpr int NRD= TR? 14: 9;
  auto rd=[&](int m,int slot,int ks,int bf){
    const LAS char* bA=lds+PA[slot]; const LAS char* bB=lds+PB[slot];
    if(!TR){
      if(m==0) fa[bf][0]=ldf(bA+adA[ks][0]);
      else if(m<=5) fb[bf][m-1]=ldf(bB+adB[ks]+(m-1)*2048);
      else fa[bf][m-5]=ldf(bA+adA[ks][m-5]);
    } else {
      if(m==0) fa[bf][0]=ldf(bA+adA[ks][0]);
      else if(m<=10){ const int c=(m-1)/2; const LAS char* a=bB+adB[c&1]+32*c+ks*10240;
        if(m&1) tl[c]=ldtr1(a); else fb[bf][c]=cat(tl[c],ldtr1(a+1280)); }
      else fa[bf][m-10]=ldf(bA+adA[ks][m-10]);
    }
  };
  #pragma unroll
  for(int m=0;m<NRD;m++) rd(m,T0&3,0,0);
  GAP();
  sfor<N>([&](auto T){ constexpr int t=T.value;
    sfor<2>([&](auto Sx){ constexpr int s=Sx.value; constexpr bool last=(s==1);
      wait_lgkm();
      if(last){ hook(T,ic<-1>{}); bar(); }
      GAP();
      constexpr int slot=(T0+t+(last?1:0))&3, ns=last?0:1;
      sfor<20>([&](auto M){ constexpr int m=M.value, fm=m/5, f0=m%5, fn=(fm&1)?4-f0:f0;
        acc[fm][fn]=mma(fb[s][fn],fa[s][fm],acc[fm][fn]);
        if(m<NRD && !(last && t==N-1)) rd(m,slot,ns,s^1);
        hook(T,ic<s*20+m>{});
        GAP();
      });
    });
  });
}
struct Qd { u32x4 a,b; u32x2 c; };
DI Qd pack_quarter(const f32x4 (&a)[5]){ Qd r;
  r.a=(u32x4){pk(a[0][0],a[0][1]),pk(a[0][2],a[0][3]),pk(a[1][0],a[1][1]),pk(a[1][2],a[1][3])};
  r.b=(u32x4){pk(a[2][0],a[2][1]),pk(a[2][2],a[2][3]),pk(a[3][0],a[3][1]),pk(a[3][2],a[3][3])};
  r.c=(u32x2){pk(a[4][0],a[4][1]),pk(a[4][2],a[4][3])}; return r; }
template<int PART> DI void put_part(const srd_t& s, unsigned base, unsigned lane, const Qd& v){
  if constexpr(PART==0) __builtin_amdgcn_raw_buffer_store_b128(v.a,s,base+lane*16,0,16);
  if constexpr(PART==1) __builtin_amdgcn_raw_buffer_store_b128(v.b,s,base+1024+lane*16,0,16);
  if constexpr(PART==2) __builtin_amdgcn_raw_buffer_store_b64(v.c,s,base+2048+lane*8,0,16); }
DI Qd get_quarter(const srd_t& s, unsigned base, unsigned lane){
  Qd r; r.a=__builtin_amdgcn_raw_buffer_load_b128(s,base+lane*16,0,16); r.b=__builtin_amdgcn_raw_buffer_load_b128(s,base+1024+lane*16,0,16);
  r.c=__builtin_amdgcn_raw_buffer_load_b64(s,base+2048+lane*8,0,16); return r;
}
DI void add_quarter(float (&t)[20], const Qd& r){
  #pragma unroll
  for(int k=0;k<4;k++){ t[2*k]+=bflo(r.a[k]); t[2*k+1]+=bfhi(r.a[k]); t[8+2*k]+=bflo(r.b[k]); t[9+2*k]+=bfhi(r.b[k]); }
  #pragma unroll
  for(int k=0;k<2;k++){ t[16+2*k]+=bflo(r.c[k]); t[17+2*k]+=bfhi(r.c[k]); }
}

DI void p2_mfma(const Prm& p, LAS char* lds, int q, int pm, int pn, unsigned w, unsigned lane, u64 tag TSP){
  const int tile=pm*4+pn;
  f32x4 acc[4][5];
  const srd_t sY=mksrd(p.ws+WS_YX), sX=mksrd(p.ws+WS_FX);
  const unsigned wr=w>>1, wc=w&1, g=lane>>4, c16=lane&15;
  const unsigned trow=pm*128+wr*64+q*16+c16, ecol=pn*160+wc*80+4*g;
  const unsigned sbase=((tile*4+q)*16+w)*2560, rbase=((tile*16+q)*4+w)*2560;
  const unsigned pY=WS_YF+((tile*4+w)*4+(lane&3))*8, pO=WS_OF+((tile*4+w)*4+(lane&3))*8;
  u64 pv=0;
  { const unsigned pH=WS_HF+(pm*16+q*4+(lane&3))*8;
    p2_loop<false,0,NY>(lds,q,w,lane,acc,[&](auto T,auto M){ constexpr int t=T.value, m=M.value;
      if constexpr(t==T_PH && m==17){ if(w==1) poll_issue(pv,p.ws,pH); }
      if constexpr(t==NY-4 && m==-1){ if(w==1){ wait_vm<0>(); poll_check(pv,p.ws,pH,tag); } } }); }
  TS(7);
  Qd yq[3], y0=pack_quarter(acc[0]);
  #pragma unroll
  for(int k=0;k<3;k++) yq[k]=pack_quarter(acc[k+1]);
  asm volatile("":"+v"(y0.a),"+v"(y0.b),"+v"(y0.c));
  GAP();
  p2_loop<true,NY,NO>(lds,q,w,lane,acc,[&](auto T,auto M){ constexpr int t=T.value, m=M.value;
    if constexpr(t<3 && (m==14||m==16||m==18)) put_part<(m-14)/2>(sY,sbase+((q+1+t)&3)*4*2560,lane,yq[t<3? t: 0]);
    if constexpr(t==4 && m==18){ wait_vm<0>(); if(lane==0) st_flag(p.ws,WS_YF+((tile*4+w)*4+q)*8,tag); }
    if constexpr(t==5 && m==38) poll_issue(pv,p.ws,pY); });
  TS(8);
  float to[20];
  #pragma unroll
  for(int k=1;k<4;k++){ const Qd v=pack_quarter(acc[k]); const unsigned b=sbase+((q+k)&3)*4*2560;
    put_part<0>(sX,b,lane,v); put_part<1>(sX,b,lane,v); put_part<2>(sX,b,lane,v); }
  #pragma unroll
  for(int k=0;k<20;k++) to[k]=acc[0][k>>2][k&3];
  TS(9);
  wait_vm<9>(); poll_check(pv,p.ws,pY,tag);
  { Qd y[3], r[3]; u32x2 xv[5];
    #pragma unroll
    for(int k=0;k<3;k++) y[k]=get_quarter(sY,rbase+((q+1+k)&3)*16*2560,lane);
    #pragma unroll
    for(int fn=0;fn<5;fn++) xv[fn]=*(const u32x2*)(p.x+(size_t)trow*D_+ecol+16*fn);
    GAP();
    wait_vm<14>(); if(lane==0) st_flag(p.ws,WS_OF+((tile*4+w)*4+q)*8,tag);
    TS(12);
    wait_flags(p.ws,pO,tag);
    TS(13);
    #pragma unroll
    for(int k=0;k<3;k++) r[k]=get_quarter(sX,rbase+((q+1+k)&3)*16*2560,lane);
    TS(10);
    float ty[20];
    #pragma unroll
    for(int k=0;k<20;k++) ty[k]=0.f;
    add_quarter(ty,y0);
    #pragma unroll
    for(int k=0;k<3;k++){ add_quarter(ty,y[k]); add_quarter(to,r[k]); }
    #pragma unroll
    for(int fn=0;fn<5;fn++){ u32x2 o;
      #pragma unroll
      for(int k=0;k<2;k++){ const float o0=to[4*fn+2*k], o1=to[4*fn+2*k+1];
        o[k]=pk(bflo(xv[fn][k])+ty[4*fn+2*k]*(o0*sigm(o0)), bfhi(xv[fn][k])+ty[4*fn+2*k+1]*(o1*sigm(o1))); }
      *(u32x2*)(p.out+(size_t)trow*D_+ecol+16*fn)=o; }
  }
  TS(11);
}
__global__ __launch_bounds__(512,2) void fnet_mlp_fused(Prm p){
  extern __shared__ __attribute__((aligned(1024))) char smem[];
  LAS char* lds=(LAS char*)smem;
  const unsigned tid=threadIdx.x, lane=tid&63, w=rfl(tid>>6);
  const int b=blockIdx.x, x8=b&7, r=b>>3;
  const int q=x8>>1, pm=(x8&1)*8+(r>>2), pn=r&3, j=q*4+pn;
  TSR(14); TS(0);
  const unsigned long long pad0=__builtin_amdgcn_s_memrealtime();
  if(w>=4){
    const unsigned lw=w-4;
    const u64 tag= lw<2? loader<3>(p,lds,pm,j,q,pn,lw,lane TSA) : loader<2>(p,lds,pm,j,q,pn,lw,lane TSA);
    TS(8);
    if(b==0 && lw==0){
      #pragma unroll 1
      for(int i=0;i<4;i++) wait_flags(p.ws,WS_ABF+(i*64+lane)*8,tag);
      if(lane==0) st_flag(p.ws,0,tag);
    }
  } else {
    const u64 tag=p1_mfma(p,lds,pm,j,w,lane TSA);
    p2_mfma(p,lds,q,pm,pn,w,lane,tag TSA);
  }
  TSR(15); TSPUT();
  {
    #pragma unroll 1
    for(int i=0;i<100000;i++){ if(__builtin_amdgcn_s_memrealtime()-pad0>=PADT) break; __builtin_amdgcn_s_sleep(1);} }
}

extern "C" void kernel_launch(void* const* d_in, const int* in_sizes, int n_in, void* d_out, int out_size, void* d_ws, size_t ws_size, hipStream_t stream) {
  Prm p; p.x=(const u16*)d_in[0]; p.wsre=(const u16*)d_in[1]; p.wsim=(const u16*)d_in[2]; p.wdre=(const u16*)d_in[3]; p.wdim=(const u16*)d_in[4];
  p.w1=(const u16*)d_in[6]; p.w2=(const u16*)d_in[7]; p.out=(u16*)d_out; p.ws=(char*)d_ws;
  (void)hipFuncSetAttribute((const void*)fnet_mlp_fused, hipFuncAttributeMaxDynamicSharedMemorySize, LDS_BYTES);
  fnet_mlp_fused<<<256,512,LDS_BYTES,stream>>>(p);
}
```

```cpp
#define PADT 0
#define PADMODE 0
#include <hip/hip_runtime.h>
#include <stdint.h>
#include <type_traits>
#include <utility>
typedef unsigned short u16;
typedef unsigned long long u64;
typedef float f32x4 __attribute__((ext_vector_type(4)));
typedef float f32x2 __attribute__((ext_vector_type(2)));
typedef __bf16 bf16x8 __attribute__((ext_vector_type(8)));
typedef __bf16 bf16x2 __attribute__((ext_vector_type(2)));
typedef short s16x4 __attribute__((ext_vector_type(4)));
typedef short s16x8 __attribute__((ext_vector_type(8)));
typedef unsigned u32x4 __attribute__((ext_vector_type(4)));
typedef unsigned u32x2 __attribute__((ext_vector_type(2)));
#define DI __device__ __forceinline__
#define GAP() __builtin_amdgcn_sched_barrier(0)
#define LAS __attribute__((address_space(3)))
#define GAS __attribute__((address_space(1)))
typedef GAS u64 gu64;
typedef GAS unsigned gu32;
typedef __amdgpu_buffer_rsrc_t srd_t;

constexpr int S_=2048, D_=640, F_=1536;
constexpr unsigned WS_ABF=8192, WS_HF=12288, WS_YF=16384, WS_OF=24576, WS_RST=65536;
constexpr unsigned WS_ABT=1u<<20, WS_H=8u<<20, WS_YX=16u<<20, WS_FX=28u<<20;
constexpr int STG1=28672, O_W=16384, ABS=4*STG1, AUX=155648, ROWM=AUX, ROWR=AUX+512, CSA=AUX+1024, CSH=AUX+2048, STG=AUX+4096, LDS_BYTES=163840;
constexpr int slot1(int i){ return (i<10? i%5: (i-10)%4)*STG1; }
constexpr int PA[4]={114688,131072,98304,20480}, PB[4]={57344,77824,0,36864}, HSTG=20480;
constexpr unsigned SPIN_MAX=40000;
#define ROTEXPR ((pn+pm)&3)
#define K0EXPR (5*((pn+pm)&1))
constexpr int NY=16, NO=6, NT=NY+NO;

#define TS(i)
#define TT(ph,t)
#define TSR(l)
#define TSPUT()
#define TSP
#define TSA

template<int I> using ic=std::integral_constant<int,I>;
template<int... I,class F> DI void sfor_(std::integer_sequence<int,I...>, F&& f){ (f(ic<I>{}),...); }
template<int N,class F> DI void sfor(F&& f){ sfor_(std::make_integer_sequence<int,N>{},f); }
DI unsigned rfl(unsigned x){ return __builtin_amdgcn_readfirstlane(x); }
DI float bflo(unsigned v){ return __uint_as_float(v<<16); }
DI float bfhi(unsigned v){ return __uint_as_float(v&0xffff0000u); }
DI unsigned pk(float lo,float hi){ f32x2 v={lo,hi}; bf16x2 b=__builtin_convertvector(v,bf16x2); return __builtin_bit_cast(unsigned,b); }
DI float sigm(float v){ return __builtin_amdgcn_rcpf(1.f+__builtin_amdgcn_exp2f(-1.4426950408889634f*v)); }
template<int N> DI void wait_vm(){ asm volatile("s_waitcnt vmcnt(%0)"::"n"(N):"memory"); }
DI void bar(){ __builtin_amdgcn_s_barrier(); }
DI void wait_lgkm(){ __builtin_amdgcn_s_waitcnt(0xC07F); }
constexpr unsigned BIAS=4096;
#define DMA1(O,X) "buffer_load_dwordx4 %" #O ", %1, %2 offen" X " lds"
#define DMA_GROUP(X) \
  if constexpr(N==1) asm volatile("s_mov_b32 m0, %0\n\ts_nop 0\n\tbuffer_load_dwordx4 %3, %1, %2 offen" X " lds" :: "s"(m0v),"s"(srd),"s"(soff),"v"(v[0]) : "memory"); \
  if constexpr(N==2) asm volatile("s_mov_b32 m0, %0\n\ts_nop 0\n\tbuffer_load_dwordx4 %3, %1, %2 offen" X " lds\n\tbuffer_load_dwordx4 %4, %1, %2 offen offset:1024" X " lds" :: "s"(m0v),"s"(srd),"s"(soff),"v"(v[0]),"v"(v[1]) : "memory"); \
  if constexpr(N==3) asm volatile("s_mov_b32 m0, %0\n\ts_nop 0\n\tbuffer_load_dwordx4 %3, %1, %2 offen" X " lds\n\tbuffer_load_dwordx4 %4, %1, %2 offen offset:1024" X " lds\n\tbuffer_load_dwordx4 %5, %1, %2 offen offset:2048" X " lds" :: "s"(m0v),"s"(srd),"s"(soff),"v"(v[0]),"v"(v[1]),"v"(v[2]) : "memory"); \
  if constexpr(N==4) asm volatile("s_mov_b32 m0, %0\n\ts_nop 0\n\tbuffer_load_dwordx4 %3, %1, %2 offen" X " lds\n\tbuffer_load_dwordx4 %4, %1, %2 offen offset:1024" X " lds\n\tbuffer_load_dwordx4 %5, %1, %2 offen offset:2048" X " lds\n\tbuffer_load_dwordx4 %6, %1, %2 offen offset:3072" X " lds" :: "s"(m0v),"s"(srd),"s"(soff),"v"(v[0]),"v"(v[1]),"v"(v[2]),"v"(v[3]) : "memory");
#define SC1S " sc1"
template<int N,bool DEV> DI void dma_group(const __amdgpu_buffer_rsrc_t& srd, const unsigned* v, unsigned soff, unsigned m0v){
  if constexpr(DEV){ DMA_GROUP(SC1S) } else { DMA_GROUP("") }
}
template<bool DEV> DI void dma5(const __amdgpu_buffer_rsrc_t& srd, const unsigned* v, unsigned soff, unsigned m0v){
  dma_group<4,DEV>(srd,v,soff,m0v); dma_group<1,DEV>(srd,v+4,soff,m0v+4096u);
}
DI srd_t mksrd(const void* p){ return __builtin_amdgcn_make_buffer_rsrc((void*)p,0,0x7ffffff0,0x00020000); }
DI srd_t mksrd_lo(const void* p){ return mksrd((const char*)p-BIAS); }
DI void st_flag(char* ws, unsigned off, u64 tag){ __hip_atomic_store((gu64*)(ws+off), tag, __ATOMIC_RELAXED, __HIP_MEMORY_SCOPE_AGENT); }
DI void st_f32(char* ws, unsigned off, float v){ __hip_atomic_store((gu32*)(ws+off), __float_as_uint(v), __ATOMIC_RELAXED, __HIP_MEMORY_SCOPE_AGENT); }
DI u64 ld_flag(char* ws, unsigned off){ return __hip_atomic_load((gu64*)(ws+off), __ATOMIC_RELAXED, __HIP_MEMORY_SCOPE_AGENT); }
DI void wait_flags(char* ws, unsigned off, u64 tag){
  #pragma unroll 1
  for(unsigned s=0;s<SPIN_MAX;s++){ const u64 v=ld_flag(ws,off); if(__all(v==tag)) break; __builtin_amdgcn_s_sleep(2); }
}
DI u64 get_tag(const char* ws){ u64 ep; asm volatile("s_load_dwordx2 %0, %1, 0x0\n\ts_waitcnt lgkmcnt(0)":"=s"(ep):"s"(ws):"memory"); return ep+1; }
DI void poll_issue(u64& v, char* ws, unsigned off){ const char* a=ws+off; asm volatile("global_load_dwordx2 %0, %1, off sc1":"=v"(v):"v"(a):"memory"); }
DI void poll_check(u64& v, char* ws, unsigned off, u64 tag){ asm volatile("":"+v"(v)); if(!__all(v==tag)) wait_flags(ws,off,tag); }
DI bf16x8 ldf(const LAS char* a){ return *(const LAS bf16x8*)a; }
DI bf16x8 ldtr(const LAS char* a, int step){
  const s16x4 lo=__builtin_amdgcn_ds_read_tr16_b64_v4i16((LAS s16x4*)a), hi=__builtin_amdgcn_ds_read_tr16_b64_v4i16((LAS s16x4*)(a+step));
  return __builtin_bit_cast(bf16x8,(s16x8)__builtin_shufflevector(lo,hi,0,1,2,3,4,5,6,7));
}
DI s16x4 ldtr1(const LAS char* a){ return __builtin_amdgcn_ds_read_tr16_b64_v4i16((LAS s16x4*)a); }
DI bf16x8 cat(s16x4 lo, s16x4 hi){ return __builtin_bit_cast(bf16x8,(s16x8)__builtin_shufflevector(lo,hi,0,1,2,3,4,5,6,7)); }
DI f32x4 mma(bf16x8 a, bf16x8 b, f32x4 c){ return __builtin_amdgcn_mfma_f32_16x16x32_bf16(a,b,c,0,0,0); }

template<int CTRL> DI float dpp_add(float v){ return v+__int_as_float(__builtin_amdgcn_update_dpp(0,__float_as_int(v),CTRL,0xf,0xf,true)); }
DI float sum16(float v){ v=dpp_add<0xB1>(v); v=dpp_add<0x4E>(v); v=dpp_add<0x141>(v); return dpp_add<0x140>(v); }

struct Prm { const u16* x; const u16* wsre; const u16* wsim; const u16* wdre; const u16* wdim; const u16* w1; const u16* w2; u16* out; char* ws; };

enum { K_P1, K_YA, K_YB, K_O, K_STAT, K_STCHK, K_ABS, K_FAB, K_PAB, K_CAB, K_HRD, K_HS, K_FH };
struct Op { int k, i, step; };
struct Sched { Op op[160]; int n; };
constexpr int TB1=20, TP2=21, TEND=TP2+NT;
#define ST_FAB 14
#define ST_FH 8
#define T_PH 10
constexpr Sched make_sched(){
  Sched s{}; auto add=[&](int k,int i,int st){ s.op[s.n].k=k; s.op[s.n].i=i; s.op[s.n].step=st; s.n++; };
  add(K_P1,0,-2); add(K_P1,1,-2); add(K_P1,2,-1); add(K_P1,3,-1); add(K_P1,4,-1);
  for(int st=0;st<20;st++){
    if(st==4) add(K_STAT,0,st);
    if(st==ST_FAB) add(K_FAB,0,st);
    if(st<=8) add(K_P1,st+5,st); else if(st>=10 && st<=15) add(K_P1,st+4,st);
    if(st==8) add(K_STCHK,0,st);
    if(st==10) add(K_ABS,0,st);
    if(st==15) add(K_YA,0,st);
    if(st==16) add(K_YA,1,st);
    if(st==18) add(K_PAB,0,st);
    if(st==19){ add(K_CAB,0,st); add(K_YB,0,st); add(K_YB,1,st); add(K_YA,2,st); add(K_YB,2,st); }
  }
  add(K_HRD,0,TB1);
  for(int t=0;t<NT;t++){ const int st=TP2+t;
    if(t==ST_FH) add(K_FH,0,st);
    if(t==0){ add(K_YA,3,st); add(K_YB,3,st); }
    if(t<6) add(K_HS,t,st);
    if(t+4<NY){ add(K_YA,t+4,st); add(K_YB,t+4,st); } else if(t+4<NT) add(K_O,t+4,st);
  }
  return s;
}
constexpr Sched SCH=make_sched();
constexpr int op_cnt(const Op& o,int nwd){ return o.k==K_P1? (o.i<10? 4+nwd: 7): o.k==K_YA? 4: o.k==K_YB? 5: o.k==K_O? 9: o.k==K_ABS? 5: (o.k==K_HS||o.k==K_PAB)? 1: 0; }
constexpr bool needed(const Op& o,int tau){
  if(tau<19 && o.k==K_P1 && o.i==tau+1) return true;
  if(tau==8 && o.k==K_STAT) return true;
  if(tau>=TB1 && tau<TEND-1){ const int t=tau-TB1; if((o.k==K_YA||o.k==K_YB||o.k==K_O) && o.i==t) return true; }
  return false; }
constexpr int last_needed(int tau){ int l=-1; for(int n=0;n<SCH.n;n++) if(SCH.op[n].step<tau && needed(SCH.op[n],tau)) l=n; return l; }
constexpr int allowed(int tau,int nwd){ const int l=last_needed(tau); if(l<0) return -1; int c=0; for(int n=l+1;n<SCH.n;n++) if(SCH.op[n].step<tau) c+=op_cnt(SCH.op[n],nwd); return c; }
constexpr int find_op(int k,int i){ for(int n=0;n<SCH.n;n++) if(SCH.op[n].k==k && SCH.op[n].i==i) return n; return 1<<20; }
constexpr bool done_by(int k,int i,int tau){ const int p=find_op(k,i); for(int t=-1;t<=tau;t++) if(last_needed(t)>=p) return true; return false; }
constexpr int step_begin(int st){ int n=0; while(n<SCH.n && SCH.op[n].step<st) n++; return n; }
static_assert(done_by(K_ABS,0,ST_FAB) && done_by(K_HS,5,TP2+ST_FH),"a flag follows its completed stores");
static_assert(T_PH<NY-4,"schedule");

template<int NWD>
DI u64 loader(const Prm& p, LAS char* lds, int pm, int j, int q, int pn, unsigned lw, unsigned lane TSP){
  const unsigned ldsb=(unsigned)(size_t)lds;
  const unsigned r=lane>>3;
  const int rot=ROTEXPR, k0=K0EXPR;
  u64 tag, pv=0;
  const srd_t sA=mksrd_lo(p.x+(size_t)pm*128*D_), sD=mksrd_lo((j<8? p.wdre: p.wdim)+(size_t)(j&7)*80*D_), sW=mksrd_lo(p.w1+j*96);
  const unsigned q0= NWD==3? 3*lw: 2*lw+2;
  unsigned vX[4], vD[NWD], vW[3];
  #pragma unroll
  for(int i=0;i<4;i++) vX[i]=(32*lw+16*(i&1)+4*(i>>1)+8*(r>>2)+(r&3))*(D_*2)+((lane&7)^((i&1)*4+(r>>1)))*16+BIAS-1024*i;
  #pragma unroll
  for(int i=0;i<NWD;i++) vD[i]=((q0+i)*8+r)*(D_*2)+((lane&7)^(((q0+i)&1)*4+(r>>1)))*16+BIAS-1024*i;
  #pragma unroll
  for(int i=0;i<3;i++){ const unsigned c=i*64+lane, row=c/12, ch=c%12; vW[i]=(16*lw+row)*(F_*2)+32*((ch>>1)^((row>>3)&1))+16*(ch&1)+BIAS-1024*i; }
  auto p1=[&](auto I){ constexpr int i=I.value; int kt=k0+(i%10); kt= kt>=10? kt-10: kt;
    dma_group<4,false>(sA,vX,kt*128u,ldsb+lw*4096u+slot1(i));
    if constexpr(i<10) dma_group<NWD,false>(sD,vD,kt*128u,ldsb+O_W+q0*1024u+slot1(i));
    else dma_group<3,false>(sW,vW,kt*(64u*F_*2),ldsb+O_W+lw*3072u+slot1(i)); };
  TS(1);
  p1(ic<0>{}); p1(ic<1>{});
  TS(2);
  tag=get_tag(p.ws);
  const srd_t sWs=mksrd_lo((q<2? p.wsre: p.wsim)+(size_t)pm*128*S_+(q&1)*1024), sT=mksrd_lo(p.ws+WS_ABT+(size_t)pn*160*8192+q*2048);
  const srd_t sH=mksrd_lo(p.ws+WS_H+((size_t)pm*48+q*12)*8192), sW2=mksrd_lo(p.w2+(size_t)q*384*D_+pn*160), sWk=mksrd(p.ws);
  unsigned vA[4], vB[5], vH[4], vW2[5];
  #pragma unroll
  for(int i=0;i<4;i++){ const unsigned c=(lane&7)^((i&1)*4+(r>>1));
    vA[i]=(32*lw+8*i+r)*(S_*2)+c*16+BIAS-1024*i; vH[i]=(c>>2)*8192+(32*lw+8*i+r)*64+(c&3)*16+BIAS-1024*i; }
  #pragma unroll
  for(int i=0;i<5;i++){ const unsigned bi=BIAS-(i<4? 1024*i: 0);
    vB[i]=((5*lw+i)*8+r)*8192+((lane&7)^(((lw+i)&1)*4+(r>>1)))*16+bi;
    const unsigned qq=i*64+lane, row=qq/20, ch=qq%20; vW2[i]=(16*lw+row)*(D_*2)+32*((ch>>1)^((row>>3)&1))+16*(ch&1)+bi; }
  const unsigned lA2=ldsb+lw*4096u, lB2=ldsb+lw*5120u;
  u32x4 hv[6];
  const unsigned pAB=WS_ABF+((((q>>1)*4+pn)*16+(q&1)*8)*2+(lane&15))*8;
  auto exec=[&](auto N){ constexpr Op o=SCH.op[N.value];
    if constexpr(o.k==K_P1) p1(ic<o.i>{});
    if constexpr(o.k==K_YA){ const int u=(o.i&~3)|((o.i+rot)&3); dma_group<4,false>(sWs,vA,u*128u,lA2+PA[o.i&3]); }
    if constexpr(o.k==K_YB){ const int u=(o.i&~3)|((o.i+rot)&3); dma5<true>(sT,vB,u*128u,lB2+PB[o.i&3]); }
    if constexpr(o.k==K_O){ int ko=o.i-NY+rot; ko= ko>=NO? ko-NO: ko;
      dma_group<4,true>(sH,vH,ko*16384u,lA2+PA[o.i&3]); dma5<false>(sW2,vW2,ko*(64u*D_*2),lB2+PB[o.i&3]); }
    if constexpr(o.k==K_STAT){ if(NWD==3 && lw==0){ const unsigned v0=WS_RST+pm*2048+lane*16, vs[2]={v0,v0}; dma_group<2,true>(sWk,vs,0,ldsb+STG); } }
    if constexpr(o.k==K_STCHK){ if(NWD==3 && lw==0){
        u32x4 a=*(const LAS u32x4*)(lds+STG+lane*16), b=*(const LAS u32x4*)(lds+STG+1024+lane*16);
        const unsigned tg=(unsigned)tag;
        if(!__all(a[1]==tg && a[3]==tg && b[1]==tg && b[3]==tg)){
          #pragma unroll 1
          for(unsigned sp=0;sp<SPIN_MAX;sp++){ asm volatile("":::"memory");
            a=__builtin_amdgcn_raw_buffer_load_b128(sWk,WS_RST+pm*2048+lane*16,0,16); b=__builtin_amdgcn_raw_buffer_load_b128(sWk,WS_RST+pm*2048+1024+lane*16,0,16);
            if(__all(a[1]==tg && a[3]==tg && b[1]==tg && b[3]==tg)) break; } }
        LAS unsigned* rm=(LAS unsigned*)(lds+ROWM); LAS unsigned* rr=(LAS unsigned*)(lds+ROWR);
        rm[lane]=a[0]; rr[lane]=a[2]; rm[64+lane]=b[0]; rr[64+lane]=b[2];
        wait_lgkm(); } }
    if constexpr(o.k==K_ABS){
      u32x4 v[5];
      #pragma unroll
      for(int n=0;n<5;n++){ const unsigned col=20*lw+4*n+(lane>>4); v[n]=*(const LAS u32x4*)(lds+ABS+col*256+(((lane&15)^(col&15))*16)); }
      #pragma unroll
      for(int n=0;n<5;n++){ const unsigned col=20*lw+4*n+(lane>>4);
        __builtin_amdgcn_raw_buffer_store_b128(v[n],sWk,WS_ABT+((j&7)*80+col)*8192+(j>>3)*4096+pm*256+(lane&15)*16,0,16); } }
    if constexpr(o.k==K_FAB){ if(lw==0 && lane==0) st_flag(p.ws,WS_ABF+((((j>>3)*4+((j&7)>>1))*16+pm)*2+(j&1))*8,tag); }
    if constexpr(o.k==K_PAB) poll_issue(pv,p.ws,pAB);
    if constexpr(o.k==K_CAB){ wait_vm<0>(); poll_check(pv,p.ws,pAB,tag); }
    if constexpr(o.k==K_HRD){
      #pragma unroll
      for(int rg=0;rg<2;rg++){ const unsigned s=(lw*2+rg)*16+(lane>>2), sw=((s&3)<<2)^(((s>>1)&1)|(((s>>3)&1)<<1));
        #pragma unroll
        for(int G=0;G<3;G++) hv[rg*3+G]=*(const LAS u32x4*)(lds+HSTG+s*256+(((4*G+(lane&3))^sw)*16)); }
      wait_lgkm(); }
    if constexpr(o.k==K_HS) __builtin_amdgcn_raw_buffer_store_b128(hv[o.i],sWk,WS_H+(pm*48+3*j+o.i%3)*8192+((lw*2+o.i/3)*16+(lane>>2))*64+(lane&3)*16,0,16);
    if constexpr(o.k==K_FH){ if(lw==0 && lane==0) st_flag(p.ws,WS_HF+(pm*16+j)*8,tag); }
  };
  sfor<TEND+1>([&](auto I){ constexpr int tau=I.value-1, al=allowed(tau,NWD);
    static_assert(al<64,"vmcnt range");
    if constexpr(al>=0) wait_vm<al>();
    if constexpr(tau==-1) TS(3);
    bar();
    if constexpr(tau==-1) TS(5); else if constexpr(tau<TB1) TT(0,tau); else if constexpr(tau==TB1) TS(4); else TT(1,tau-TP2);
    constexpr int b0=step_begin(tau), b1=step_begin(tau+1);
    sfor<b1-b0>([&](auto M){ exec(ic<b0+M.value>{}); });
  });
  return tag;
}
DI u64 p1_mfma(const Prm& p, LAS char* lds, int pm, int j, unsigned w, unsigned lane TSP){
  u64 tag;
  const unsigned wr=w>>1, wc=w&1, g=lane>>4, c16=lane&15;
  if(w<2){ const unsigned row=pm*128+j*8+w*4+g; u32x4 v[5]; float s=0;
    #pragma unroll
    for(int i=0;i<5;i++) v[i]=*(const u32x4*)(p.x+(size_t)row*D_+(c16+16*i)*8);
    tag=get_tag(p.ws);
    #pragma unroll
    for(int i=0;i<5;i++)
      #pragma unroll
      for(int k=0;k<4;k++) s+=bflo(v[i][k])+bfhi(v[i][k]);
    const float m=sum16(s)*(1.f/D_); float q2=0;
    #pragma unroll
    for(int i=0;i<5;i++)
      #pragma unroll
      for(int k=0;k<4;k++){ const float a=bflo(v[i][k])-m, c=bfhi(v[i][k])-m; q2+=a*a+c*c; }
    const float rs=1.0f/sqrtf(sum16(q2)*(1.f/D_)+1e-5f);
    if(c16<2) st_flag(p.ws,WS_RST+row*16+c16*8, ((u64)(unsigned)tag<<32)|__float_as_uint(c16==0? m: rs));
  } else tag=get_tag(p.ws);
  TS(1);
  const u32x4 one4={0x3F803F80u,0x3F803F80u,0x3F803F80u,0x3F803F80u}; const bf16x8 ones=__builtin_bit_cast(bf16x8,one4);
  const unsigned rho=3*wr;
  unsigned aA[2], aB[2][5], hA[2], hB[2][3];
  #pragma unroll
  for(int s=0;s<2;s++){
    { const unsigned ch=((4*(s^wc)+g)^((lane>>1)&7))*16; aA[s]=(32*w+c16)*128+ch;
      #pragma unroll
      for(int f=0;f<5;f++){ const unsigned fr=(f+rho)%5; aB[s][f]=O_W+(fr*16+c16)*128+ch; } }
    { const unsigned ks=s^wr, ch=((4*ks+g)^((lane>>1)&7))*16; hA[s]=(wr*64+c16)*128+ch;
      #pragma unroll
      for(int f=0;f<3;f++) hB[s][f]=O_W+192*(8*g+(c16>>2))+8*(lane&3)+32*((3*wc+f)^(g&1))+ks*6144; }
  }
  f32x4 acc[2][5], csa[3];
  #pragma unroll
  for(int k=0;k<5;k++) acc[0][k]=acc[1][k]=(f32x4){0.f,0.f,0.f,0.f};
  csa[0]=csa[1]=csa[2]=(f32x4){0.f,0.f,0.f,0.f};
  bf16x8 fa[2][2], fb[2][5], ga[2][4], gb[2][3]; s16x4 tl[3];
  auto rdA=[&](int m,const LAS char* base,int s){
    if(m==0) fa[s][0]=ldf(base+aA[s]); else if(m<=5) fb[s][m-1]=ldf(base+aB[s][m-1]); else fa[s][1]=ldf(base+aA[s]+2048); };
  auto rdH=[&](int m,const LAS char* base,int s){
    if(m==0) ga[s][0]=ldf(base+hA[s]);
    else if(m<=6){ const int c=(m-1)/2; if(m&1) tl[c]=ldtr1(base+hB[s][c]); else gb[s][c]=cat(tl[c],ldtr1(base+hB[s][c]+768)); }
    else ga[s][m-6]=ldf(base+hA[s]+(m-6)*2048); };
  bar();
  TS(2);
  #pragma unroll
  for(int m=0;m<7;m++) rdA(m,lds,0);
  GAP();
  const unsigned s0=32*w+8*g;
  f32x4 m0,m1,r0,r1; float c[5];
  sfor<10>([&](auto T){ constexpr int t=T.value;
    #pragma unroll
    for(int s=0;s<2;s++){
      const bool last=(s==1);
      if(last && t==9){ LAS float* cw=(LAS float*)(lds+CSA)+wc*80;
        if(g==0){ cw[rho*16+c16]=csa[0][0]; cw[(rho+1)*16+c16]=csa[1][0]; if(wr==0) cw[32+c16]=csa[2][0]; } }
      wait_lgkm();
      if(last) bar();
      GAP();
      const LAS char* base=lds+slot1(last? t+1: t);
      if(last && t==9){ const LAS float* cs=(const LAS float*)(lds+CSA);
        #pragma unroll
        for(int f=0;f<5;f++){ const unsigned fr=(f+rho)%5; c[f]=cs[fr*16+c16]+cs[80+fr*16+c16]; }
        m0=*(const LAS f32x4*)(lds+ROWM+s0*4); m1=*(const LAS f32x4*)(lds+ROWM+s0*4+16); r0=*(const LAS f32x4*)(lds+ROWR+s0*4); r1=*(const LAS f32x4*)(lds+ROWR+s0*4+16); }
      constexpr int NM= 13;
      #pragma unroll
      for(int m=0;m<NM;m++){
        if(!last){ if(m<3) csa[m]=mma(ones,fb[s][m],csa[m]); else { const int mm=m-3, fm=mm/5, f0=mm%5, fn=fm?4-f0:f0; acc[fm][fn]=mma(fa[s][fm],fb[s][fn],acc[fm][fn]); } }
        else if(m<10){ const int fm=m/5, f0=m%5, fn=fm?4-f0:f0; acc[fm][fn]=mma(fa[s][fm],fb[s][fn],acc[fm][fn]); }
        if(last && t==9){ if(m<10) rdH(m,base,0); }
        else if(m<7) rdA(m,base,s^1);
        GAP();
      }
    }
  });
  TS(3);
  { const float sg= j<8? 1.f: -1.f;
    r0*=sg; r1*=sg;
    #pragma unroll
    for(int f=0;f<5;f++){ const unsigned col=((f+rho)%5)*16+c16;
      const f32x4 a=(acc[0][f]-m0*c[f])*r0, b=(acc[1][f]-m1*c[f])*r1;
      const u32x4 o={pk(a[0],a[1]),pk(a[2],a[3]),pk(b[0],b[1]),pk(b[2],b[3])};
      *(LAS u32x4*)(lds+ABS+col*256+(((4*w+g)^c16)*16))=o; }
  }
  GAP();
  TS(4);
  f32x4 hac[4][3], csh[3], c4[3];
  #pragma unroll
  for(int k=0;k<3;k++){ csh[k]=(f32x4){0.f,0.f,0.f,0.f};
    #pragma unroll
    for(int i=0;i<4;i++) hac[i][k]=(f32x4){0.f,0.f,0.f,0.f}; }
  float hm[4], hr[4]; unsigned hb[4], hx[4];
  sfor<10>([&](auto T){ constexpr int t=T.value, i=10+t;
    #pragma unroll
    for(int s=0;s<2;s++){
      const bool last=(s==1);
      if(last && t==9){ LAS float* cw=(LAS float*)(lds+CSH)+wr*96+wc*48;
        if(c16==0){
          #pragma unroll
          for(int f=0;f<3;f++) *(LAS f32x4*)(cw+16*f+4*g)=csh[f]; } }
      wait_lgkm();
      if(last) bar();
      GAP();
      const LAS char* base=lds+slot1(last? (t==9? i: i+1): i);
      if(last && t==9){ const LAS float* cs=(const LAS float*)(lds+CSH)+wc*48;
        #pragma unroll
        for(int f=0;f<3;f++) c4[f]=*(const LAS f32x4*)(cs+16*f+4*g)+*(const LAS f32x4*)(cs+96+16*f+4*g);
        #pragma unroll
        for(int fm=0;fm<4;fm++){ const unsigned s=wr*64+32*(fm>>1)+16*(c16>>3)+8*((c16>>2)&1)+4*(fm&1)+(c16&3);
          hm[fm]=((const LAS float*)(lds+ROWM))[s]; hr[fm]=((const LAS float*)(lds+ROWR))[s];
          hb[fm]=HSTG+s*256+(g&1)*8; hx[fm]=((g>>1)^((s&3)<<2)^(((s>>1)&1)|(((s>>3)&1)<<1)))*16; } }
      #pragma unroll
      for(int m=0;m<15;m++){
        if(!last){ if(m<3) csh[m]=mma(gb[s][m],ones,csh[m]); else { const int mm=m-3, fm=mm/3, f0=mm%3, fn=(fm&1)?2-f0:f0; hac[fm][fn]=mma(gb[s][fn],ga[s][fm],hac[fm][fn]); } }
        else if(m<12){ const int fm=m/3, f0=m%3, fn=(fm&1)?2-f0:f0; hac[fm][fn]=mma(gb[s][fn],ga[s][fm],hac[fm][fn]); }
        if(m<10 && !(last && t==9)) rdH(m,base,s^1);
        GAP();
      }
    }
  });
  TS(5);
  #pragma unroll
  for(int fm=0;fm<4;fm++){ const float zr=hr[fm]*-1.4426950408889634f;
    #pragma unroll
    for(int f=0;f<3;f++){ const f32x4 v=hac[fm][f]-c4[f]*hm[fm], a=v*hr[fm], z=v*zr; f32x4 e;
      #pragma unroll
      for(int k=0;k<4;k++) e[k]=__builtin_amdgcn_exp2f(z[k]);
      e+=1.f;
      #pragma unroll
      for(int k=0;k<4;k++) e[k]=__builtin_amdgcn_rcpf(e[k]);
      const f32x4 h=a*e; const u32x2 o={pk(h[0],h[1]),pk(h[2],h[3])};
      *(LAS u32x2*)(lds+hb[fm]+(hx[fm]^((3*wc+f)*32)))=o; } }
  wait_lgkm();
  bar();
  TS(6);
  return tag;
}

template<bool TR,int T0,int N,class Hook>
DI void p2_loop(LAS char* lds, int q, unsigned w, unsigned lane, f32x4 (&acc)[4][5], Hook hook){
  const unsigned wr=w>>1, wc=w&1, g=lane>>4, c16=lane&15;
  unsigned adA[2][4], adB[2];
  #pragma unroll
  for(int s=0;s<2;s++){ const unsigned ch=((4*s+g)^((lane>>1)&7))*16;
    #pragma unroll
    for(int fm=0;fm<4;fm++) adA[s][fm]=(wr*64+((fm+q)&3)*16+c16)*128+ch;
    if(!TR) adB[s]=(wc*80+c16)*128+ch;
    else { const int sgn=((wc+s)&1)? -32: 32; adB[s]=320*(8*g+(c16>>2))+8*(lane&3)+160*wc+sgn*(int)(g&1); } }
  #pragma unroll
  for(int i=0;i<4;i++)
    #pragma unroll
    for(int k=0;k<5;k++) acc[i][k]=(f32x4){0.f,0.f,0.f,0.f};
  bf16x8 fa[2][4], fb[2][5]; s16x4 tl[5];
  constexpr int NRD= TR? 14: 9;
  auto rd=[&](int m,int slot,int ks,int bf){
    const LAS char* bA=lds+PA[slot]; const LAS char* bB=lds+PB[slot];
    if(!TR){
      if(m==0) fa[bf][0]=ldf(bA+adA[ks][0]);
      else if(m<=5) fb[bf][m-1]=ldf(bB+adB[ks]+(m-1)*2048);
      else fa[bf][m-5]=ldf(bA+adA[ks][m-5]);
    } else {
      if(m==0) fa[bf][0]=ldf(bA+adA[ks][0]);
      else if(m<=10){ const int c=(m-1)/2; const LAS char* a=bB+adB[c&1]+32*c+ks*10240;
        if(m&1) tl[c]=ldtr1(a); else fb[bf][c]=cat(tl[c],ldtr1(a+1280)); }
      else fa[bf][m-10]=ldf(bA+adA[ks][m-10]);
    }
  };
  #pragma unroll
  for(int m=0;m<NRD;m++) rd(m,T0&3,0,0);
  GAP();
  sfor<N>([&](auto T){ constexpr int t=T.value;
    sfor<2>([&](auto Sx){ constexpr int s=Sx.value; constexpr bool last=(s==1);
      wait_lgkm();
      if(last){ hook(T,ic<-1>{}); bar(); }
      GAP();
      constexpr int slot=(T0+t+(last?1:0))&3, ns=last?0:1;
      sfor<20>([&](auto M){ constexpr int m=M.value, fm=m/5, f0=m%5, fn=(fm&1)?4-f0:f0;
        acc[fm][fn]=mma(fb[s][fn],fa[s][fm],acc[fm][fn]);
        if(m<NRD && !(last && t==N-1)) rd(m,slot,ns,s^1);
        hook(T,ic<s*20+m>{});
        GAP();
      });
    });
  });
}
struct Qd { u32x4 a,b; u32x2 c; };
DI Qd pack_quarter(const f32x4 (&a)[5]){ Qd r;
  r.a=(u32x4){pk(a[0][0],a[0][1]),pk(a[0][2],a[0][3]),pk(a[1][0],a[1][1]),pk(a[1][2],a[1][3])};
  r.b=(u32x4){pk(a[2][0],a[2][1]),pk(a[2][2],a[2][3]),pk(a[3][0],a[3][1]),pk(a[3][2],a[3][3])};
  r.c=(u32x2){pk(a[4][0],a[4][1]),pk(a[4][2],a[4][3])}; return r; }
template<int PART> DI void put_part(const srd_t& s, unsigned base, unsigned lane, const Qd& v){
  if constexpr(PART==0) __builtin_amdgcn_raw_buffer_store_b128(v.a,s,base+lane*16,0,16);
  if constexpr(PART==1) __builtin_amdgcn_raw_buffer_store_b128(v.b,s,base+1024+lane*16,0,16);
  if constexpr(PART==2) __builtin_amdgcn_raw_buffer_store_b64(v.c,s,base+2048+lane*8,0,16); }
DI Qd get_quarter(const srd_t& s, unsigned base, unsigned lane){
  Qd r; r.a=__builtin_amdgcn_raw_buffer_load_b128(s,base+lane*16,0,16); r.b=__builtin_amdgcn_raw_buffer_load_b128(s,base+1024+lane*16,0,16);
  r.c=__builtin_amdgcn_raw_buffer_load_b64(s,base+2048+lane*8,0,16); return r;
}
DI void add_quarter(float (&t)[20], const Qd& r){
  #pragma unroll
  for(int k=0;k<4;k++){ t[2*k]+=bflo(r.a[k]); t[2*k+1]+=bfhi(r.a[k]); t[8+2*k]+=bflo(r.b[k]); t[9+2*k]+=bfhi(r.b[k]); }
  #pragma unroll
  for(int k=0;k<2;k++){ t[16+2*k]+=bflo(r.c[k]); t[17+2*k]+=bfhi(r.c[k]); }
}

DI void p2_mfma(const Prm& p, LAS char* lds, int q, int pm, int pn, unsigned w, unsigned lane, u64 tag TSP){
  const int tile=pm*4+pn;
  f32x4 acc[4][5];
  const srd_t sY=mksrd(p.ws+WS_YX), sX=mksrd(p.ws+WS_FX);
  const unsigned wr=w>>1, wc=w&1, g=lane>>4, c16=lane&15;
  const unsigned trow=pm*128+wr*64+q*16+c16, ecol=pn*160+wc*80+4*g;
  const unsigned sbase=((tile*4+q)*16+w)*2560, rbase=((tile*16+q)*4+w)*2560;
  const unsigned pY=WS_YF+((tile*4+w)*4+(lane&3))*8, pO=WS_OF+((tile*4+w)*4+(lane&3))*8;
  u64 pv=0;
  { const unsigned pH=WS_HF+(pm*16+q*4+(lane&3))*8;
    p2_loop<false,0,NY>(lds,q,w,lane,acc,[&](auto T,auto M){ constexpr int t=T.value, m=M.value;
      if constexpr(t==T_PH && m==17){ if(w==1) poll_issue(pv,p.ws,pH); }
      if constexpr(t==NY-4 && m==-1){ if(w==1){ wait_vm<0>(); poll_check(pv,p.ws,pH,tag); } } }); }
  TS(7);
  Qd yq[3], y0=pack_quarter(acc[0]);
  #pragma unroll
  for(int k=0;k<3;k++) yq[k]=pack_quarter(acc[k+1]);
  asm volatile("":"+v"(y0.a),"+v"(y0.b),"+v"(y0.c));
  GAP();
  p2_loop<true,NY,NO>(lds,q,w,lane,acc,[&](auto T,auto M){ constexpr int t=T.value, m=M.value;
    if constexpr(t<3 && (m==14||m==16||m==18)) put_part<(m-14)/2>(sY,sbase+((q+1+t)&3)*4*2560,lane,yq[t<3? t: 0]);
    if constexpr(t==4 && m==18){ wait_vm<0>(); if(lane==0) st_flag(p.ws,WS_YF+((tile*4+w)*4+q)*8,tag); }
    if constexpr(t==5 && m==38) poll_issue(pv,p.ws,pY); });
  TS(8);
  float to[20];
  #pragma unroll
  for(int k=1;k<4;k++){ const Qd v=pack_quarter(acc[k]); const unsigned b=sbase+((q+k)&3)*4*2560;
    put_part<0>(sX,b,lane,v); put_part<1>(sX,b,lane,v); put_part<2>(sX,b,lane,v); }
  #pragma unroll
  for(int k=0;k<20;k++) to[k]=acc[0][k>>2][k&3];
  TS(9);
  wait_vm<9>(); poll_check(pv,p.ws,pY,tag);
  { Qd y[3], r[3]; u32x2 xv[5];
    #pragma unroll
    for(int k=0;k<3;k++) y[k]=get_quarter(sY,rbase+((q+1+k)&3)*16*2560,lane);
    #pragma unroll
    for(int fn=0;fn<5;fn++) xv[fn]=*(const u32x2*)(p.x+(size_t)trow*D_+ecol+16*fn);
    GAP();
    wait_vm<14>(); if(lane==0) st_flag(p.ws,WS_OF+((tile*4+w)*4+q)*8,tag);
    TS(12);
    wait_flags(p.ws,pO,tag);
    TS(13);
    #pragma unroll
    for(int k=0;k<3;k++) r[k]=get_quarter(sX,rbase+((q+1+k)&3)*16*2560,lane);
    TS(10);
    float ty[20];
    #pragma unroll
    for(int k=0;k<20;k++) ty[k]=0.f;
    add_quarter(ty,y0);
    #pragma unroll
    for(int k=0;k<3;k++){ add_quarter(ty,y[k]); add_quarter(to,r[k]); }
    #pragma unroll
    for(int fn=0;fn<5;fn++){ u32x2 o;
      #pragma unroll
      for(int k=0;k<2;k++){ const float o0=to[4*fn+2*k], o1=to[4*fn+2*k+1];
        o[k]=pk(bflo(xv[fn][k])+ty[4*fn+2*k]*(o0*sigm(o0)), bfhi(xv[fn][k])+ty[4*fn+2*k+1]*(o1*sigm(o1))); }
      __builtin_amdgcn_raw_buffer_store_b64(o,mksrd(p.out),(trow*D_+ecol+16*fn)*2,0,16); }
  }
  TS(11);
}
__global__ __launch_bounds__(512,2) void fnet_mlp_fused(Prm p){
  extern __shared__ __attribute__((aligned(1024))) char smem[];
  LAS char* lds=(LAS char*)smem;
  const unsigned tid=threadIdx.x, lane=tid&63, w=rfl(tid>>6);
  const int b=blockIdx.x, x8=b&7, r=b>>3;
  const int q=x8>>1, pm=(x8&1)*8+(r>>2), pn=r&3, j=q*4+pn;
  TSR(14); TS(0);
  unsigned long long pad0=__builtin_amdgcn_s_memrealtime();
  if(PADMODE==1 && tid==256) st_flag(p.ws,32768+b*8,pad0);
  if(w>=4){
    const unsigned lw=w-4;
    const u64 tag= lw<2? loader<3>(p,lds,pm,j,q,pn,lw,lane TSA) : loader<2>(p,lds,pm,j,q,pn,lw,lane TSA);
    TS(8);
    if(b==0 && lw==0){
      #pragma unroll 1
      for(int i=0;i<4;i++) wait_flags(p.ws,WS_ABF+(i*64+lane)*8,tag);
      if(lane==0) st_flag(p.ws,0,tag);
    }
  } else {
    const u64 tag=p1_mfma(p,lds,pm,j,w,lane TSA);
    p2_mfma(p,lds,q,pm,pn,w,lane,tag TSA);
  }
  TSR(15); TSPUT();
  {
    #pragma unroll 1
    for(int i=0;i<100000;i++){ if(__builtin_amdgcn_s_memrealtime()-pad0>=PADT) break; __builtin_amdgcn_s_sleep(1);} }
}

extern "C" void kernel_launch(void* const* d_in, const int* in_sizes, int n_in, void* d_out, int out_size, void* d_ws, size_t ws_size, hipStream_t stream) {
  Prm p; p.x=(const u16*)d_in[0]; p.wsre=(const u16*)d_in[1]; p.wsim=(const u16*)d_in[2]; p.wdre=(const u16*)d_in[3]; p.wdim=(const u16*)d_in[4];
  p.w1=(const u16*)d_in[6]; p.w2=(const u16*)d_in[7]; p.out=(u16*)d_out; p.ws=(char*)d_ws;
  (void)hipFuncSetAttribute((const void*)fnet_mlp_fused, hipFuncAttributeMaxDynamicSharedMemorySize, LDS_BYTES);
  fnet_mlp_fused<<<256,512,LDS_BYTES,stream>>>(p);
}
```

```cpp
#define STAMP 2
#include <cstdio>
#include <hip/hip_runtime.h>
#include <stdint.h>
#include <type_traits>
#include <utility>
typedef unsigned short u16;
typedef unsigned long long u64;
typedef float f32x4 __attribute__((ext_vector_type(4)));
typedef float f32x2 __attribute__((ext_vector_type(2)));
typedef __bf16 bf16x8 __attribute__((ext_vector_type(8)));
typedef __bf16 bf16x2 __attribute__((ext_vector_type(2)));
typedef short s16x4 __attribute__((ext_vector_type(4)));
typedef short s16x8 __attribute__((ext_vector_type(8)));
typedef unsigned u32x4 __attribute__((ext_vector_type(4)));
typedef unsigned u32x2 __attribute__((ext_vector_type(2)));
#define DI __device__ __forceinline__
#define GAP() __builtin_amdgcn_sched_barrier(0)
#define LAS __attribute__((address_space(3)))
#define GAS __attribute__((address_space(1)))
typedef GAS u64 gu64;
typedef GAS unsigned gu32;
typedef __amdgpu_buffer_rsrc_t srd_t;

constexpr int S_=2048, D_=640, F_=1536;
constexpr unsigned WS_ABF=8192, WS_HF=12288, WS_YF=16384, WS_RST=65536;
constexpr unsigned WS_ABT=1u<<20, WS_H=8u<<20, WS_YX=16u<<20, WS_W2=30u<<20;
constexpr int XS(int i){ return i*16384; }
constexpr int WS_(int i){ return 98304+i*12288; }
constexpr int ABS1=XS(4), ABS2=155648, AUX=159744, STG=AUX, ROWM=AUX+2048, ROWR=ROWM+512, CSX=ROWM+1024, LDS_BYTES=163840;
constexpr int xab(int i){ return XS(i%6); }
constexpr int wab(int i){ return WS_(i%5); }
constexpr int xh(int n){ return n<5? XS((5+n)%6): n==5? XS(5): XS(n-6); }
constexpr int wh(int n){ constexpr int m[10]={0,1,2,3,0,4,1,2,3,4}; return WS_(m[n]); }
constexpr int PA[5]={XS(4),XS(0),XS(1),XS(2),XS(3)}, PB[4]={81920,102400,122880,143360};
constexpr int HST0=PA[4], HST1=PB[3], YOWN=PA[0];
constexpr unsigned SPIN_MAX=40000;
#define WSOFF 0
#define ROTEXPR ((pn+pm)&3)
#define YG 4
#define OROT rot
#define YOFF ((pm+q)&3)
#define OOFF ((pm+q)&3)
#define K0EXPR ((pm+3*j)%10)
constexpr int NY=16, NO=12, NT=NY+NO;
constexpr int slotA(int t){ return t<NY? PA[t%5]: PA[1+(t-NY)%4]; }
constexpr int slotB(int t){ return PB[t&3]; }

#define TSX(v,l) do{ const unsigned t_=(unsigned)__builtin_amdgcn_s_memtime(); asm volatile("v_writelane_b32 %0, %1, %2":"+v"(v):"s"(t_),"n"(l)); }while(0)
#define TSR(l) do{ const unsigned t_=(unsigned)__builtin_amdgcn_s_memrealtime(); asm volatile("v_writelane_b32 %0, %1, %2":"+v"(tsv):"s"(t_),"n"(l)); }while(0)
#define TSPUT() do{ GAS unsigned* o_=((GAS unsigned*)(p.ws-WSOFF+(200u<<20)))+(blockIdx.x*8+(threadIdx.x>>6))*256+(threadIdx.x&63); o_[0]=tsv; o_[64]=tsw; o_[128]=tsx; o_[192]=tsb; }while(0)
#define TS(i)
#define TB(tau)
#define TW(tau)
#define TX(tau)
#define TSP , unsigned& tsv, unsigned& tsw, unsigned& tsx, unsigned& tsb
#define TSA , tsv, tsw, tsx, tsb

template<int I> using ic=std::integral_constant<int,I>;
template<int... I,class F> DI void sfor_(std::integer_sequence<int,I...>, F&& f){ (f(ic<I>{}),...); }
template<int N,class F> DI void sfor(F&& f){ sfor_(std::make_integer_sequence<int,N>{},f); }
DI unsigned rfl(unsigned x){ return __builtin_amdgcn_readfirstlane(x); }
DI float bflo(unsigned v){ return __uint_as_float(v<<16); }
DI float bfhi(unsigned v){ return __uint_as_float(v&0xffff0000u); }
DI unsigned pk(float lo,float hi){ f32x2 v={lo,hi}; bf16x2 b=__builtin_convertvector(v,bf16x2); return __builtin_bit_cast(unsigned,b); }
DI float sigm(float v){ return __builtin_amdgcn_rcpf(1.f+__builtin_amdgcn_exp2f(-1.4426950408889634f*v)); }
template<int N> DI void wait_vm(){ asm volatile("s_waitcnt vmcnt(%0)"::"n"(N):"memory"); }
DI void bar(){ __builtin_amdgcn_s_barrier(); }
DI void wait_lgkm(){ __builtin_amdgcn_s_waitcnt(0xC07F); }
constexpr unsigned BIAS=4096;
#define DMA1(O,X) "buffer_load_dwordx4 %" #O ", %1, %2 offen" X " lds"
#define DMA_GROUP(X) \
  if constexpr(N==1) asm volatile("s_mov_b32 m0, %0\n\ts_nop 0\n\tbuffer_load_dwordx4 %3, %1, %2 offen" X " lds" :: "s"(m0v),"s"(srd),"s"(soff),"v"(v[0]) : "memory"); \
  if constexpr(N==2) asm volatile("s_mov_b32 m0, %0\n\ts_nop 0\n\tbuffer_load_dwordx4 %3, %1, %2 offen" X " lds\n\tbuffer_load_dwordx4 %4, %1, %2 offen offset:1024" X " lds" :: "s"(m0v),"s"(srd),"s"(soff),"v"(v[0]),"v"(v[1]) : "memory"); \
  if constexpr(N==3) asm volatile("s_mov_b32 m0, %0\n\ts_nop 0\n\tbuffer_load_dwordx4 %3, %1, %2 offen" X " lds\n\tbuffer_load_dwordx4 %4, %1, %2 offen offset:1024" X " lds\n\tbuffer_load_dwordx4 %5, %1, %2 offen offset:2048" X " lds" :: "s"(m0v),"s"(srd),"s"(soff),"v"(v[0]),"v"(v[1]),"v"(v[2]) : "memory"); \
  if constexpr(N==4) asm volatile("s_mov_b32 m0, %0\n\ts_nop 0\n\tbuffer_load_dwordx4 %3, %1, %2 offen" X " lds\n\tbuffer_load_dwordx4 %4, %1, %2 offen offset:1024" X " lds\n\tbuffer_load_dwordx4 %5, %1, %2 offen offset:2048" X " lds\n\tbuffer_load_dwordx4 %6, %1, %2 offen offset:3072" X " lds" :: "s"(m0v),"s"(srd),"s"(soff),"v"(v[0]),"v"(v[1]),"v"(v[2]),"v"(v[3]) : "memory");
#define SC1S " sc1"
template<int N,int POL> DI void dma_group(const __amdgpu_buffer_rsrc_t& srd, const unsigned* v, unsigned soff, unsigned m0v){
  if constexpr(POL==1){ DMA_GROUP(SC1S) } else if constexpr(POL==2){ DMA_GROUP(" nt") } else { DMA_GROUP("") }
}
template<int POL> DI void dma5(const __amdgpu_buffer_rsrc_t& srd, const unsigned* v, unsigned soff, unsigned m0v){
  dma_group<4,POL>(srd,v,soff,m0v); dma_group<1,POL>(srd,v+4,soff,m0v+4096u);
}
#define NTX 0
#define NTD 0
#define NT1 0
#define NTS 0
#define NTSF NTS
#define NT2 0
DI srd_t mksrd(const void* p){ return __builtin_amdgcn_make_buffer_rsrc((void*)p,0,0x7ffffff0,0x00020000); }
DI srd_t mksrd_lo(const void* p){ return mksrd((const char*)p-BIAS); }
DI void st_flag(char* ws, unsigned off, u64 tag){ __hip_atomic_store((gu64*)(ws+off), tag, __ATOMIC_RELAXED, __HIP_MEMORY_SCOPE_AGENT); }
DI void st_f32(char* ws, unsigned off, float v){ __hip_atomic_store((gu32*)(ws+off), __float_as_uint(v), __ATOMIC_RELAXED, __HIP_MEMORY_SCOPE_AGENT); }
DI u64 ld_flag(char* ws, unsigned off){ return __hip_atomic_load((gu64*)(ws+off), __ATOMIC_RELAXED, __HIP_MEMORY_SCOPE_AGENT); }
DI void wait_flags(char* ws, unsigned off, u64 tag){
  #pragma unroll 1
  for(unsigned s=0;s<SPIN_MAX;s++){ const u64 v=ld_flag(ws,off); if(__all(v==tag)) break; __builtin_amdgcn_s_sleep(2); }
}
DI u64 get_tag(const char* ws){ u64 ep; asm volatile("s_load_dwordx2 %0, %1, 0x0\n\ts_waitcnt lgkmcnt(0)":"=s"(ep):"s"(ws):"memory"); return ep+1; }
DI void poll_issue(u64& v, char* ws, unsigned off){ const char* a=ws+off; asm volatile("global_load_dwordx2 %0, %1, off sc1":"=v"(v):"v"(a):"memory"); }
DI void poll_check(u64& v, char* ws, unsigned off, u64 tag){ asm volatile("":"+v"(v)); if(!__all(v==tag)) wait_flags(ws,off,tag); }
DI const LAS char* at(unsigned reg,int off){ return (const LAS char*)(size_t)reg+off; }
#define PIN(x) asm volatile("":"+v"(x))
DI bf16x8 ldf(const LAS char* a){ return *(const LAS bf16x8*)a; }
DI bf16x8 ldtr(const LAS char* a, int step){
  const s16x4 lo=__builtin_amdgcn_ds_read_tr16_b64_v4i16((LAS s16x4*)a), hi=__builtin_amdgcn_ds_read_tr16_b64_v4i16((LAS s16x4*)(a+step));
  return __builtin_bit_cast(bf16x8,(s16x8)__builtin_shufflevector(lo,hi,0,1,2,3,4,5,6,7));
}
DI s16x4 ldtr1(const LAS char* a){ return __builtin_amdgcn_ds_read_tr16_b64_v4i16((LAS s16x4*)a); }
DI bf16x8 cat(s16x4 lo, s16x4 hi){ return __builtin_bit_cast(bf16x8,(s16x8)__builtin_shufflevector(lo,hi,0,1,2,3,4,5,6,7)); }
DI f32x4 mma(bf16x8 a, bf16x8 b, f32x4 c){ return __builtin_amdgcn_mfma_f32_16x16x32_bf16(a,b,c,0,0,0); }

template<int CTRL> DI float dpp_add(float v){ return v+__int_as_float(__builtin_amdgcn_update_dpp(0,__float_as_int(v),CTRL,0xf,0xf,true)); }
DI float sum16(float v){ v=dpp_add<0xB1>(v); v=dpp_add<0x4E>(v); v=dpp_add<0x141>(v); return dpp_add<0x140>(v); }

struct Prm { const u16* x; const u16* wsre; const u16* wsim; const u16* wdre; const u16* wdim; const u16* w1; const u16* w2; u16* out; char* ws; };

enum { K_X, K_WD, K_XR, K_W1, K_YA, K_YB, K_OA, K_OB, K_STAT, K_STCHK, K_ABS, K_FAB, K_PAB, K_CAB, K_HRD, K_HS, K_FH, K_PF, K_W2L, K_W2S };
struct Op { int k, i, step; };
struct Sched { Op op[200]; int n; };
constexpr int TB1=20, TP2=21, TEND=TP2+NT;
#define ST_FAB 15
#define ST_PAB 18
#define ST_YA0 16
#define ST_YA1 17
#define ST_YA2 19
#define ST_YA3 19
#define PFU 4
#define PFY 0
#define ST_PFY 9
#define ST_PFW 100
#define ST_PF1 100
#define ST_W2L 6
#define ST_W2S 9
#define ST_FH 8
#define T_PH 10
#define YPAIR 0
constexpr Sched make_sched(){
  Sched s{}; auto add=[&](int k,int i,int st){ s.op[s.n].k=k; s.op[s.n].i=i; s.op[s.n].step=st; s.n++; };
#define PRO 1
  if(PRO==0){ add(K_X,0,-2); add(K_WD,0,-2); add(K_X,1,-2); add(K_WD,1,-2); add(K_X,2,-1); add(K_WD,2,-1); add(K_X,3,-1); }
  if(PRO==1){ add(K_X,0,-2); add(K_WD,0,-2); add(K_X,1,-2); add(K_X,2,-2); add(K_WD,1,-2); add(K_WD,2,-1); add(K_X,3,-1); }
  if(PRO==2){ add(K_X,0,-2); add(K_WD,0,-2); add(K_X,1,-2); add(K_WD,1,-2); add(K_WD,2,-2); add(K_X,2,-1); add(K_X,3,-1); }
  for(int st=0;st<20;st++){
    if(st==4) add(K_STAT,0,st);
    if(st==ST_FAB) add(K_FAB,0,st);
    if(st==ST_PAB) add(K_PAB,0,st);
    if(st<=6) add(K_WD,st+3,st);
    if(st<=5) add(K_X,st+4,st);
    if(st>=5 && st<=8) add(K_W1,st-5,st);
    if(st==ST_W2L) add(K_W2L,0,st);
    if(st==8) add(K_STCHK,0,st);
    if(st==ST_W2S) add(K_W2S,0,st);
    if(st==ST_PFY && PFY>0) add(K_PF,0,st);
    if(st==ST_PFY && PFY>1) add(K_PF,1,st);
    if(st==ST_PFW) add(K_PF,2,st);
    if(st==ST_PF1) add(K_PF,3,st);
    if(st==10){ add(K_W1,4,st); add(K_ABS,0,st); }
    if(st==11){ add(K_XR,0,st); add(K_W1,5,st); }
    if(st>=12 && st<=15){ add(K_XR,st-11,st); add(K_W1,st-6,st); }
    if(st==ST_YA0) add(K_YA,0,st);
    if(st==ST_YA1) add(K_YA,1,st);
    if(st==19){ add(K_CAB,0,st); add(K_YB,0,st); add(K_YB,1,st);
      if(ST_YA2==19) add(K_YA,2,st); add(K_YB,2,st); if(ST_YA3==19) add(K_YA,3,st); }
    if(st==ST_YA2 && st!=19) add(K_YA,2,st);
    if(st==ST_YA3 && st!=19) add(K_YA,3,st);
  }
  add(K_HRD,0,TB1);
  for(int t=0;t<NT;t++){ const int st=TP2+t;
    if(t==ST_FH) add(K_FH,0,st);
    if(t==0){ add(K_YB,3,st); if(YPAIR) add(K_YA,5-1,st); else add(K_YA,4,st); }
    if(t<6 && !(YPAIR && t==0)) add(K_HS,t,st);
    if(YPAIR==0){
      if(t+4<NY) add(K_YB,t+4,st);
      if(t+5<NY) add(K_YA,t+5,st); }
    if(YPAIR==1){
      if(t+4<NY) add(K_YB,t+4,st);
      if(t==0) add(K_YA,5,st);
      if(t>0 && (t&1)==0){ if(t+4<NY) add(K_YA,t+4,st); if(t+5<NY) add(K_YA,t+5,st); } }
    if(YPAIR==2){
      if(t==0) add(K_YA,5,st);
      if(t>0 && (t&1)==0){ if(t+4<NY) add(K_YA,t+4,st); if(t+5<NY) add(K_YA,t+5,st); }
      if(t+4<NY) add(K_YB,t+4,st); }
    if(YPAIR && t==0) add(K_HS,0,st);
    if(t+4>=NY && t+4<NT){ add(K_OA,t+4,st); add(K_OB,t+4,st); }
  }
  return s;
}
constexpr Sched SCH=make_sched();
constexpr int op_cnt(const Op& o,int nwd){
  return o.k==K_WD? nwd: o.k==K_W1? 3: (o.k==K_X||o.k==K_XR||o.k==K_YA||o.k==K_OA)? 4: (o.k==K_YB||o.k==K_OB||o.k==K_ABS)? 5: (o.k==K_W2L||o.k==K_W2S)? 2: (o.k==K_HS||o.k==K_PAB||(o.k==K_PF && o.i<2))? 1: 0; }
constexpr int first_use(const Op& o){
  return (o.k==K_X||o.k==K_WD)? o.i-1: o.k==K_W1? o.i+9: o.k==K_XR? o.i+14: (o.k==K_YA||o.k==K_YB||o.k==K_OA||o.k==K_OB)? TB1+o.i: o.k==K_STAT? 8: 1000; }
constexpr int last_use(const Op& o){
  return o.k==K_X? (o.i>=5? o.i+5: o.i): o.k==K_WD? o.i: o.k==K_W1? o.i+10: o.k==K_XR? o.i+15: (o.k==K_YA||o.k==K_YB||o.k==K_OA||o.k==K_OB)? TP2+o.i: o.k==K_STAT? 9: -1000; }
constexpr int lds_lo(const Op& o){
  return o.k==K_X? xab(o.i): o.k==K_WD? wab(o.i): o.k==K_W1? wh(o.i): o.k==K_XR? xh(o.i+5): (o.k==K_YA||o.k==K_OA)? slotA(o.i): (o.k==K_YB||o.k==K_OB)? slotB(o.i): o.k==K_STAT? STG: -1; }
constexpr int lds_sz(const Op& o){
  return o.k==K_WD? 10240: o.k==K_W1? 12288: (o.k==K_X||o.k==K_XR||o.k==K_YA||o.k==K_OA)? 16384: (o.k==K_YB||o.k==K_OB)? 20480: o.k==K_STAT? 2048: 0; }
constexpr bool needed(const Op& o,int tau){ return first_use(o)==tau; }
constexpr int last_needed(int tau){ int l=-1; for(int n=0;n<SCH.n;n++) if(SCH.op[n].step<tau && needed(SCH.op[n],tau)) l=n; return l; }
constexpr int allowed(int tau,int nwd){ const int l=last_needed(tau); if(l<0) return -1; int c=0; for(int n=l+1;n<SCH.n;n++) if(SCH.op[n].step<tau) c+=op_cnt(SCH.op[n],nwd); return c; }
constexpr int find_op(int k,int i){ for(int n=0;n<SCH.n;n++) if(SCH.op[n].k==k && SCH.op[n].i==i) return n; return 1<<20; }
constexpr bool done_by(int k,int i,int tau){ const int p=find_op(k,i); for(int t=-1;t<=tau;t++) if(last_needed(t)>=p) return true; return false; }
constexpr int behind(int k0,int k1,int nwd){ int c=0; for(int n=find_op(k0,0)+1;n<find_op(k1,0);n++) c+=op_cnt(SCH.op[n],nwd); return c; }
constexpr int step_begin(int st){ int n=0; while(n<SCH.n && SCH.op[n].step<st) n++; return n; }
struct Reg { int lo, sz, from, to; };
constexpr Reg STAGED[7]={ {ABS1,16384,9,11}, {ABS2,4096,9,11}, {HST0,16384,19,TP2}, {HST1,16384,19,TP2}, {YOWN,10240,TP2+NY-1,1000},
                          {ROWM,1024,8,TB1}, {CSX,1024,7,TB1} };
constexpr bool sched_ok(){
  for(int n=0;n<SCH.n;n++){ const Op& a=SCH.op[n];
    if(n && a.step<SCH.op[n-1].step) return false;
    if(lds_sz(a)==0) continue;
    if(a.step>=first_use(a)) return false;
    for(int m=0;m<SCH.n;m++){ const Op& b=SCH.op[m]; if(m==n || lds_sz(b)==0) continue;
      if(lds_lo(a)<lds_lo(b)+lds_sz(b) && lds_lo(b)<lds_lo(a)+lds_sz(a) && a.step<last_use(b) && b.step<last_use(a)) return false; }
    for(const Reg& r: STAGED) if(lds_lo(a)<r.lo+r.sz && r.lo<lds_lo(a)+lds_sz(a) && a.step<r.to && r.from<last_use(a)) return false; }
  for(int k: {K_X,K_WD,K_W1}) for(int i=0;i<10;i++) if(find_op(k,i)>=SCH.n) return false;
  for(int i=0;i<5;i++) if(find_op(K_XR,i)>=SCH.n) return false;
  for(int i=0;i<NT;i++) if(find_op(i<NY? K_YA: K_OA,i)>=SCH.n || find_op(i<NY? K_YB: K_OB,i)>=SCH.n) return false;
  return true; }
static_assert(sched_ok(),"LDS regions are reused only behind the barrier that ends their last read");
static_assert(find_op(K_W2L,0)<find_op(K_W2S,0) && find_op(K_W2S,0)<find_op(K_ABS,0),"the a/b flag also covers the re-laid part of w2");
static_assert(done_by(K_ABS,0,ST_FAB) && done_by(K_HS,5,TP2+ST_FH),"a flag follows its completed stores");
static_assert(find_op(K_CAB,0)<find_op(K_YB,0) && find_op(K_PAB,0)<find_op(K_CAB,0),"a/b tiles are loaded behind the check of their flags");
static_assert(T_PH<NY-4 && SCH.op[find_op(K_OA,NY)].step>=TP2+NY-4,"h tiles are loaded behind the check of their flags");

template<int NWD>
DI u64 loader(const Prm& p, LAS char* lds, int pm, int j, int q, int pn, unsigned lw, unsigned lane TSP){
  const unsigned ldsb=(unsigned)(size_t)lds;
  const unsigned r=lane>>3;
  const int rot=ROTEXPR, k0=K0EXPR, orot=OROT, yoff=YOFF, ooff=OOFF;
  u64 tag, pv=0;
  const srd_t sA=mksrd_lo(p.x+(size_t)pm*128*D_), sD=mksrd_lo((j<8? p.wdre: p.wdim)+(size_t)(j&7)*80*D_), sW=mksrd_lo(p.w1+j*96);
  const unsigned q0= NWD==3? 3*lw: 2*lw+2;
  unsigned vX[4], vD[NWD], vW[3];
  #pragma unroll
  for(int i=0;i<4;i++) vX[i]=(32*lw+16*(i&1)+4*(i>>1)+8*(r>>2)+(r&3))*(D_*2)+((lane&7)^((i&1)*4+(r>>1)))*16+BIAS-1024*i;
  #pragma unroll
  for(int i=0;i<NWD;i++) vD[i]=((q0+i)*8+r)*(D_*2)+((lane&7)^(((q0+i)&1)*4+(r>>1)))*16+BIAS-1024*i;
  #pragma unroll
  for(int i=0;i<3;i++){ const unsigned c=i*64+lane, row=c/12, ch=c%12; vW[i]=(16*lw+row)*(F_*2)+32*((ch>>1)^((row>>3)&1))+16*(ch&1)+BIAS-1024*i; }
  auto ktile=[&](int i){ const int kt=k0+i; return kt>=10? kt-10: kt; };
  auto ldx=[&](int i,int dst){ dma_group<4,NTX>(sA,vX,ktile(i)*128u,ldsb+lw*4096u+dst); };
  auto ldwd=[&](int i){ dma_group<NWD,NTD>(sD,vD,ktile(i)*128u,ldsb+q0*1024u+wab(i)); };
  constexpr int NPRE=step_begin(-1);
  TS(1);
  sfor<NPRE>([&](auto N){ constexpr Op o=SCH.op[N.value]; static_assert(o.k==K_X||o.k==K_WD,"prologue");
    if constexpr(o.k==K_X) ldx(o.i,xab(o.i)); else ldwd(o.i); });
  TS(2);
  tag=get_tag(p.ws);
  const srd_t sWs=mksrd_lo((q<2? p.wsre: p.wsim)+(size_t)pm*128*S_+(q&1)*1024), sT=mksrd_lo(p.ws+WS_ABT+(size_t)pn*160*8192+q*2048);
  const srd_t sH=mksrd_lo(p.ws+WS_H+(size_t)pm*48*8192+(q>>1)*4096), sW2=mksrd_lo(p.ws+WS_W2+(2*pn+(q&1))*245760), sWk=mksrd(p.ws);
  unsigned vA[4], vB[5], vH[4], vW2[5];
  #pragma unroll
  for(int i=0;i<4;i++){ const unsigned c=(lane&7)^((i&1)*4+(r>>1));
    vA[i]=(32*lw+8*i+r)*(S_*2)+c*16+BIAS-1024*i;
    vH[i]=(2*(lw>>1)+(c>>2))*8192+(32*(lw&1)+8*i+r)*64+(c&3)*16+BIAS-1024*i; }
  #pragma unroll
  for(int i=0;i<5;i++){ const unsigned bi=BIAS-(i<4? 1024*i: 0);
    vB[i]=((5*lw+i)*8+r)*8192+((lane&7)^(((lw+i)&1)*4+(r>>1)))*16+bi;
    vW2[i]=lw*5120+lane*16+BIAS+(i<4? 0: 4096); }
  u32x4 wv[2]; unsigned wsrc[2], wdst[2];
  #pragma unroll
  for(int i=0;i<2;i++){ const unsigned cc=i*60+(lane<60? lane: 59), rho=(2*pm+(j&1))*48+12*lw+cc/10, ch=cc%10, row=rho^(((rho>>3)&1)<<2), sl=j>>1;
    wsrc[i]=row*(D_*2)+((sl>>1)*160+(sl&1)*80)*2+ch*16; wdst[i]=WS_W2+sl*245760+rho*160+ch*16; }
  const srd_t sW2s=mksrd(p.w2);
  const unsigned lA2=ldsb+lw*4096u, lB2=ldsb+lw*5120u;
  u32x4 hv[6]; unsigned pfv=0;
  const unsigned pAB=WS_ABF+((((q>>1)*4+pn)*16+(q&1)*8)*2+(lane&15))*8;
  auto exec=[&](auto N){ constexpr Op o=SCH.op[N.value];
    if constexpr(o.k==K_X) ldx(o.i,xab(o.i));
    if constexpr(o.k==K_XR) ldx(o.i,xh(o.i+5));
    if constexpr(o.k==K_WD) ldwd(o.i);
    if constexpr(o.k==K_W1) dma_group<3,NT1>(sW,vW,ktile(o.i>=5? o.i-5: o.i+5)*(64u*F_*2),ldsb+lw*3072u+wh(o.i));
    if constexpr(o.k==K_YA){ const int i2=(o.i+yoff)&15, u=(i2&~(YG-1))|((i2+rot)&(YG-1));
      if constexpr(NTSF!=NTS){ if(o.i<=(YPAIR?1:0) || (i2&(YG-1))<=(YPAIR?1:0)) dma_group<4,NTSF>(sWs,vA,u*128u,lA2+slotA(o.i)); else dma_group<4,NTS>(sWs,vA,u*128u,lA2+slotA(o.i)); }
      else dma_group<4,NTS>(sWs,vA,u*128u,lA2+slotA(o.i)); }
    if constexpr(o.k==K_YB){ const int i2=(o.i+yoff)&15, u=(i2&~(YG-1))|((i2+rot)&(YG-1)); dma5<1>(sT,vB,u*128u,lB2+slotB(o.i)); }
    if constexpr(o.k==K_OA){ int i2=o.i-NY+ooff; i2= i2>=NO? i2-NO: i2; const int u=(i2&~3)|((i2+orot)&3); dma_group<4,1>(sH,vH,u*32768u,lA2+slotA(o.i)); }
    if constexpr(o.k==K_OB){ int i2=o.i-NY+ooff; i2= i2>=NO? i2-NO: i2; const int u=(i2&~3)|((i2+orot)&3); dma5<1>(sW2,vW2,u*20480u,lB2+slotB(o.i)); }
    if constexpr(o.k==K_STAT){ if(NWD==3 && lw==0){ const unsigned v0=WS_RST+pm*2048+lane*16, vs[2]={v0,v0}; dma_group<2,1>(sWk,vs,0,ldsb+STG); } }
    if constexpr(o.k==K_STCHK){ if(NWD==3 && lw==0){
        u32x4 a=*(const LAS u32x4*)(lds+STG+lane*16), b=*(const LAS u32x4*)(lds+STG+1024+lane*16);
        const unsigned tg=(unsigned)tag;
        if(!__all(a[1]==tg && a[3]==tg && b[1]==tg && b[3]==tg)){
          #pragma unroll 1
          for(unsigned sp=0;sp<SPIN_MAX;sp++){ asm volatile("":::"memory");
            a=__builtin_amdgcn_raw_buffer_load_b128(sWk,WS_RST+pm*2048+lane*16,0,16); b=__builtin_amdgcn_raw_buffer_load_b128(sWk,WS_RST+pm*2048+1024+lane*16,0,16);
            if(__all(a[1]==tg && a[3]==tg && b[1]==tg && b[3]==tg)) break; } }
        LAS unsigned* rm=(LAS unsigned*)(lds+ROWM); LAS unsigned* rr=(LAS unsigned*)(lds+ROWR);
        rm[lane]=a[0]; rr[lane]=a[2]; rm[64+lane]=b[0]; rr[64+lane]=b[2];
        wait_lgkm(); } }
    if constexpr(o.k==K_ABS){
      u32x4 v[5];
      #pragma unroll
      for(int n=0;n<5;n++){ const unsigned col=20*lw+4*n+(lane>>4); v[n]=*(const LAS u32x4*)(lds+(20*lw+4*n<64? ABS1: ABS2-64*256)+col*256+(((lane&15)^(col&15))*16)); }
      #pragma unroll
      for(int n=0;n<5;n++){ const unsigned col=20*lw+4*n+(lane>>4);
        __builtin_amdgcn_raw_buffer_store_b128(v[n],sWk,WS_ABT+((j&7)*80+col)*8192+(j>>3)*4096+pm*256+(lane&15)*16,0,16); } }
    if constexpr(o.k==K_FAB){ TSR(16); if(lw==0 && lane==0) st_flag(p.ws,WS_ABF+((((j>>3)*4+((j&7)>>1))*16+pm)*2+(j&1))*8,tag); }
    if constexpr(o.k==K_PF){
      const char* a;
      if constexpr(o.i<2) a=(const char*)((q<2? p.wsre: p.wsim)+(size_t)(pm*128+32*pn+8*lw+(lane>>3))*S_+(q&1)*1024+((PFU+8*o.i+(lane&7))&15)*64);
      else if constexpr(o.i==2) a=(const char*)p.w2+((size_t)blockIdx.x*64+lane)*120;
      else a=(const char*)p.w1+((size_t)blockIdx.x*64+lane)*120;
      if(o.i<2 || lw==3) asm volatile("global_load_dword %0, %1, off":"+v"(pfv):"v"(a):"memory"); }
    if constexpr(o.k==K_W2L){
      #pragma unroll
      for(int i=0;i<2;i++) asm volatile("buffer_load_dwordx4 %0, %1, %2, 0 offen":"=v"(wv[i]):"v"(wsrc[i]),"s"(sW2s):"memory"); }
    if constexpr(o.k==K_W2S){ wait_vm<behind(K_W2L,K_W2S,NWD)>(); asm volatile("":"+v"(wv[0]),"+v"(wv[1]));
      #pragma unroll
      for(int i=0;i<2;i++) __builtin_amdgcn_raw_buffer_store_b128(wv[i],sWk,wdst[i],0,16); }
    if constexpr(o.k==K_PAB) poll_issue(pv,p.ws,pAB);
    if constexpr(o.k==K_CAB){ wait_vm<behind(K_PAB,K_CAB,NWD)>(); poll_check(pv,p.ws,pAB,tag); TSR(17); }
    if constexpr(o.k==K_HRD){
      #pragma unroll
      for(int rg=0;rg<2;rg++){ const unsigned s=(lw*2+rg)*16+(lane>>2), sw=((s&3)<<2)^(((s>>1)&1)|(((s>>3)&1)<<1));
        #pragma unroll
        for(int G=0;G<3;G++) hv[rg*3+G]=*(const LAS u32x4*)(lds+(lw<2? HST0: HST1-64*256)+s*256+(((4*G+(lane&3))^sw)*16)); }
      wait_lgkm(); }
    if constexpr(o.k==K_HS) __builtin_amdgcn_raw_buffer_store_b128(hv[o.i],sWk,WS_H+(pm*48+3*j+o.i%3)*8192+((lw*2+o.i/3)*16+(lane>>2))*64+(lane&3)*16,0,16);
    if constexpr(o.k==K_FH){ if(lw==0 && lane==0) st_flag(p.ws,WS_HF+(pm*16+j)*8,tag); }
  };
  sfor<TEND+1>([&](auto I){ constexpr int tau=I.value-1, al=allowed(tau,NWD);
    static_assert(al<64,"vmcnt range");
    TW(tau);
    if constexpr(al>=0) wait_vm<al>();
    TX(tau);
    bar();
    TB(tau);
    constexpr int b0=step_begin(tau), b1=step_begin(tau+1);
    sfor<b1-b0>([&](auto M){ exec(ic<b0+M.value>{}); });
  });
  asm volatile(""::"v"(pfv));
  return tag;
}
DI u64 p1_mfma(const Prm& p, LAS char* lds, int pm, int j, unsigned w, unsigned lane TSP){
  u64 tag;
  const unsigned wr=w>>1, wc=w&1, g=lane>>4, c16=lane&15;
  if(w<2){ const unsigned row=pm*128+j*8+w*4+g; u32x4 v[5]; float s=0;
    #pragma unroll
    for(int i=0;i<5;i++) v[i]=*(const u32x4*)(p.x+(size_t)row*D_+(c16+16*i)*8);
    tag=get_tag(p.ws);
    #pragma unroll
    for(int i=0;i<5;i++)
      #pragma unroll
      for(int k=0;k<4;k++) s+=bflo(v[i][k])+bfhi(v[i][k]);
    const float m=sum16(s)*(1.f/D_); float q2=0;
    #pragma unroll
    for(int i=0;i<5;i++)
      #pragma unroll
      for(int k=0;k<4;k++){ const float a=bflo(v[i][k])-m, c=bfhi(v[i][k])-m; q2+=a*a+c*c; }
    const float rs=1.0f/sqrtf(sum16(q2)*(1.f/D_)+1e-5f);
    if(c16<2) st_flag(p.ws,WS_RST+row*16+c16*8, ((u64)(unsigned)tag<<32)|__float_as_uint(c16==0? m: rs));
  } else tag=get_tag(p.ws);
  TS(1);
  const u32x4 one4={0x3F803F80u,0x3F803F80u,0x3F803F80u,0x3F803F80u}; const bf16x8 ones=__builtin_bit_cast(bf16x8,one4);
  const unsigned rho=3*wr;
  const unsigned lb=(unsigned)(size_t)lds;
  unsigned aA[2][2], aB[2][5], hA[2][2], hB[2][3], aS[5], aC[5], hb[4], hx[4], hs[4];
  #pragma unroll
  for(int s=0;s<2;s++){
    { const unsigned ch=((4*(s^wc)+g)^((lane>>1)&7))*16; aA[0][s]=lb+(32*w+c16)*128+ch; aA[1][s]=aA[0][s]+65536;
      #pragma unroll
      for(int f=0;f<5;f++){ const unsigned fr=(f+rho)%5; aB[s][f]=lb+WS_(0)+(fr*16+c16)*128+ch; } }
    { const unsigned ks=s^wr, ch=((4*ks+g)^((lane>>1)&7))*16; hA[0][s]=lb+(wr*64+c16)*128+ch; hA[1][s]=hA[0][s]+65536;
      #pragma unroll
      for(int f=0;f<3;f++) hB[s][f]=lb+WS_(0)+192*(8*g+(c16>>2))+8*(lane&3)+32*((3*wc+f)^(g&1))+ks*6144; }
  }
  #pragma unroll
  for(int f=0;f<5;f++){ const unsigned fr=(f+rho)%5, col=fr*16+c16;
    aS[f]=lb+(fr<4? ABS1: ABS2-64*256)+col*256+(((4*w+g)^c16)*16); aC[f]=lb+CSX+col*4; }
  #pragma unroll
  for(int fm=0;fm<4;fm++){ const unsigned s=wr*64+32*(fm>>1)+16*(c16>>3)+8*((c16>>2)&1)+4*(fm&1)+(c16&3);
    hs[fm]=lb+ROWM+s*4; hb[fm]=lb+(wr? HST1-64*256: HST0)+s*256+(g&1)*8; hx[fm]=((g>>1)^((s&3)<<2)^(((s>>1)&1)|(((s>>3)&1)<<1)))*16; }
  unsigned aR=lb+ROWM+(32*w+8*g)*4, aH=lb+CSX+(wc*48+4*g)*4;
  #pragma unroll
  for(int s=0;s<2;s++){ PIN(aA[0][s]); PIN(aA[1][s]); PIN(hA[0][s]); PIN(hA[1][s]);
    #pragma unroll
    for(int f=0;f<5;f++) PIN(aB[s][f]);
    #pragma unroll
    for(int f=0;f<3;f++) PIN(hB[s][f]); }
  #pragma unroll
  for(int f=0;f<5;f++){ PIN(aS[f]); PIN(aC[f]); }
  #pragma unroll
  for(int fm=0;fm<4;fm++){ PIN(hs[fm]); PIN(hb[fm]); PIN(hx[fm]); }
  PIN(aR); PIN(aH);
  f32x4 acc[2][5], csa[3];
  #pragma unroll
  for(int k=0;k<5;k++) acc[0][k]=acc[1][k]=(f32x4){0.f,0.f,0.f,0.f};
  csa[0]=csa[1]=csa[2]=(f32x4){0.f,0.f,0.f,0.f};
  bf16x8 fa[2][2], fb[2][5], ga[2][4], gb[2][3]; s16x4 tl[3];
  auto rdA=[&](int m,int i,int s){
    const int xw=xab(i)>=65536, xo=xab(i)-65536*xw, wo=wab(i)-WS_(0);
    if(m==0) fa[s][0]=ldf(at(aA[xw][s],xo)); else if(m<=5) fb[s][m-1]=ldf(at(aB[s][m-1],wo)); else fa[s][1]=ldf(at(aA[xw][s],xo+2048)); };
  auto rdH=[&](int m,int n,int s){
    const int xw=xh(n)>=65536, xo=xh(n)-65536*xw, wo=wh(n)-WS_(0);
    if(m==0) ga[s][0]=ldf(at(hA[xw][s],xo));
    else if(m<=6){ const int c=(m-1)/2; if(m&1) tl[c]=ldtr1(at(hB[s][c],wo)); else gb[s][c]=cat(tl[c],ldtr1(at(hB[s][c],wo+768))); }
    else ga[s][m-6]=ldf(at(hA[xw][s],xo+(m-6)*2048)); };
  bar();
  TS(2); TSR(20);
  #pragma unroll
  for(int m=0;m<7;m++) rdA(m,0,0);
  GAP();
  f32x4 m0,m1,r0,r1; float c[5];
  sfor<10>([&](auto T){ constexpr int t=T.value;
    #pragma unroll
    for(int s=0;s<2;s++){
      const bool last=(s==1);
      if(last && t==9){
        #pragma unroll
        for(int f=0;f<3;f++) *(LAS float*)at(aC[f]+wc*320,0)=csa[f][0]; }
      wait_lgkm();
      if(last) bar();
      GAP();
      if(last && t==9){
        #pragma unroll
        for(int f=0;f<5;f++) c[f]=*(const LAS float*)at(aC[f],0)+*(const LAS float*)at(aC[f],320);
        m0=*(const LAS f32x4*)at(aR,0); m1=*(const LAS f32x4*)at(aR,16); r0=*(const LAS f32x4*)at(aR,512); r1=*(const LAS f32x4*)at(aR,528); }
      constexpr int NM= 13;
      #pragma unroll
      for(int m=0;m<NM;m++){
        if(!last){ if(m<3) csa[m]=mma(ones,fb[s][m],csa[m]); else { const int mm=m-3, fm=mm/5, f0=mm%5, fn=fm?4-f0:f0; acc[fm][fn]=mma(fa[s][fm],fb[s][fn],acc[fm][fn]); } }
        else if(m<10){ const int fm=m/5, f0=m%5, fn=fm?4-f0:f0; acc[fm][fn]=mma(fa[s][fm],fb[s][fn],acc[fm][fn]); }
        if(last && t==9){ if(m<10) rdH(m,0,0); }
        else if(m<7) rdA(m,last? t+1: t,s^1);
        GAP();
      }
    }
  });
  TS(3);
  { const float sg= j<8? 1.f: -1.f;
    r0*=sg; r1*=sg;
    #pragma unroll
    for(int f=0;f<5;f++){
      const f32x4 a=(acc[0][f]-m0*c[f])*r0, b=(acc[1][f]-m1*c[f])*r1;
      const u32x4 o={pk(a[0],a[1]),pk(a[2],a[3]),pk(b[0],b[1]),pk(b[2],b[3])};
      *(LAS u32x4*)at(aS[f],0)=o; }
  }
  GAP();
  TS(4); TSR(16);
  f32x4 hac[4][3], csh[3], c4[3];
  #pragma unroll
  for(int k=0;k<3;k++){ csh[k]=(f32x4){0.f,0.f,0.f,0.f};
    #pragma unroll
    for(int i=0;i<4;i++) hac[i][k]=(f32x4){0.f,0.f,0.f,0.f}; }
  float hm[4], hr[4];
  sfor<10>([&](auto T){ constexpr int t=T.value;
    #pragma unroll
    for(int s=0;s<2;s++){
      const bool last=(s==1);
      if(last && t==9){
        #pragma unroll
        for(int f=0;f<3;f++) *(LAS f32x4*)at(aH+wr*384,64*f)=csh[f]; }
      wait_lgkm();
      if(last) bar();
      GAP();
      if(last && t==9){
        #pragma unroll
        for(int f=0;f<3;f++) c4[f]=*(const LAS f32x4*)at(aH,64*f)+*(const LAS f32x4*)at(aH,384+64*f);
        #pragma unroll
        for(int fm=0;fm<4;fm++){ hm[fm]=*(const LAS float*)at(hs[fm],0); hr[fm]=*(const LAS float*)at(hs[fm],512); } }
      #pragma unroll
      for(int m=0;m<15;m++){
        if(!last){ if(m<3) csh[m]=mma(gb[s][m],ones,csh[m]); else { const int mm=m-3, fm=mm/3, f0=mm%3, fn=(fm&1)?2-f0:f0; hac[fm][fn]=mma(gb[s][fn],ga[s][fm],hac[fm][fn]); } }
        else if(m<12){ const int fm=m/3, f0=m%3, fn=(fm&1)?2-f0:f0; hac[fm][fn]=mma(gb[s][fn],ga[s][fm],hac[fm][fn]); }
        if(m<10 && !(last && t==9)) rdH(m,last? t+1: t,s^1);
        GAP();
      }
    }
  });
  TS(5);
  #pragma unroll
  for(int fm=0;fm<4;fm++){ const float zr=hr[fm]*-1.4426950408889634f;
    #pragma unroll
    for(int f=0;f<3;f++){ const f32x4 v=hac[fm][f]-c4[f]*hm[fm], a=v*hr[fm], z=v*zr; f32x4 e;
      #pragma unroll
      for(int k=0;k<4;k++) e[k]=__builtin_amdgcn_exp2f(z[k]);
      e+=1.f;
      #pragma unroll
      for(int k=0;k<4;k++) e[k]=__builtin_amdgcn_rcpf(e[k]);
      const f32x4 h=a*e; const u32x2 o={pk(h[0],h[1]),pk(h[2],h[3])};
      *(LAS u32x2*)at(hb[fm]+(hx[fm]^((3*wc+f)*32)),0)=o; } }
  wait_lgkm();
  bar();
  TS(6); TSR(17);
  return tag;
}

template<class Hook>
DI void y_loop(LAS char* lds, unsigned w, unsigned lane, f32x4 (&acc)[4][5], Hook hook){
  const unsigned wr=w>>1, wc=w&1, g=lane>>4, c16=lane&15;
  unsigned adA[2], adB[2];
  #pragma unroll
  for(int s=0;s<2;s++){ const unsigned ch=((4*s+g)^((lane>>1)&7))*16; adA[s]=(wr*64+c16)*128+ch; adB[s]=(wc*80+c16)*128+ch; }
  #pragma unroll
  for(int i=0;i<4;i++)
    #pragma unroll
    for(int k=0;k<5;k++) acc[i][k]=(f32x4){0.f,0.f,0.f,0.f};
  bf16x8 fa[2][4], fb[2][5];
  auto rd=[&](int m,int t,int ks,int bf){
    const LAS char* bA=lds+slotA(t); const LAS char* bB=lds+slotB(t);
    if(m==0) fa[bf][0]=ldf(bA+adA[ks]);
    else if(m<=5) fb[bf][m-1]=ldf(bB+adB[ks]+(m-1)*2048);
    else fa[bf][m-5]=ldf(bA+adA[ks]+(m-5)*2048); };
  #pragma unroll
  for(int m=0;m<9;m++) rd(m,0,0,0);
  GAP();
  sfor<NY>([&](auto T){ constexpr int t=T.value;
    sfor<2>([&](auto Sx){ constexpr int s=Sx.value; constexpr bool last=(s==1);
      wait_lgkm();
      if(last){ hook(T,ic<-1>{}); bar(); }
      GAP();
      sfor<20>([&](auto M){ constexpr int m=M.value, fm=m/5, f0=m%5, fn=(fm&1)?4-f0:f0;
        acc[fm][fn]=mma(fb[s][fn],fa[s][fm],acc[fm][fn]);
        if(m<9 && !(last && t==NY-1)) rd(m,last? t+1: t,last? 0: 1,s^1);
        hook(T,ic<s*20+m>{});
        GAP();
      });
    });
  });
}
struct GFrag { bf16x8 a[2][2], b[2][3]; };
struct GAddr { unsigned a[2][2], b[3][2]; };
DI GAddr gate_addr(unsigned w, unsigned lane){
  const unsigned wr=w>>1, wc=w&1, g=lane>>4, c16=lane&15; GAddr r;
  #pragma unroll
  for(int ks=0;ks<2;ks++)
    #pragma unroll
    for(int a=0;a<2;a++) r.a[ks][a]=(32*wr+16*(a^wc)+c16)*128+((4*ks+g)^((lane>>1)&7))*16;
  #pragma unroll
  for(int f=0;f<3;f++){ const unsigned b0=160*(8*g+(c16>>2))+8*(lane&3)+32*(wc? 4-f: f); r.b[f][0]=b0+(g&1)*640; r.b[f][1]=b0+640-(g&1)*640; }
  return r; }
DI void gate_rd(int m, const LAS char* lds, const GAddr& ad, int t, int h, GFrag& fr, s16x4 (&tl)[3]){
  const int ks=m/8, i=m%8; const LAS char* bA=lds+slotA(t)+h*8192; const LAS char* bB=lds+slotB(t)+(h*64+ks*32)*160;
  if(i==0) fr.a[ks][0]=ldf(bA+ad.a[ks][0]);
  else if(i<=6){ const int f=(i-1)/2; if(i&1) tl[f]=ldtr1(bB+ad.b[f][0]); else fr.b[ks][f]=cat(tl[f],ldtr1(bB+ad.b[f][1])); }
  else fr.a[ks][1]=ldf(bA+ad.a[ks][1]); }
template<class Hook>
DI void gate_loop(LAS char* lds, const GAddr& ad, GFrag (&fr)[2], f32x4 (&acc)[5], Hook hook){
  s16x4 tl[3];
  #pragma unroll
  for(int k=0;k<5;k++) acc[k]=(f32x4){0.f,0.f,0.f,0.f};
  sfor<NO>([&](auto T){ constexpr int t=NY+T.value;
    sfor<2>([&](auto Sx){ constexpr int s=Sx.value; constexpr bool last=(s==1);
      wait_lgkm();
      if(last){ hook(T,ic<-1>{}); bar(); }
      GAP();
      sfor<10>([&](auto M){ constexpr int m=M.value, ks=m/5, i=m%5, a=(i==2||i==3)? 1: 0, f= i==4? 2: (i&1);
        acc[i]=mma(fr[s].b[ks][f],fr[s].a[ks][a],acc[i]);
        if constexpr(!(last && t==NT-1)){
          if constexpr(m<6){ gate_rd(2*m,lds,ad,last? t+1: t,last? 0: 1,fr[s^1],tl); gate_rd(2*m+1,lds,ad,last? t+1: t,last? 0: 1,fr[s^1],tl); }
          else gate_rd(m+6,lds,ad,last? t+1: t,last? 0: 1,fr[s^1],tl); }
        hook(T,ic<s*10+m>{});
        GAP();
      });
    });
  });
}
struct Qd { u32x4 a,b; u32x2 c; };
DI u32x2 pk4(const f32x4& v){ return (u32x2){pk(v[0],v[1]),pk(v[2],v[3])}; }
template<int W> DI Qd pack_part(const f32x4 (&y)[4][5]){
  constexpr int wr=W>>1, wc=W&1, r0=2*wr+wc, r1=2*wr+1-wc, c0=wc? 4: 0, c1=wc? 3: 1;
  const u32x2 p0=pk4(y[r0][c0]), p1=pk4(y[r0][c1]), p2=pk4(y[r1][c0]), p3=pk4(y[r1][c1]);
  Qd r; r.a=(u32x4){p0[0],p0[1],p1[0],p1[1]}; r.b=(u32x4){p2[0],p2[1],p3[0],p3[1]}; r.c=pk4(y[r0][2]); return r; }
DI void add_part(float (&t)[20], const Qd& r){
  #pragma unroll
  for(int k=0;k<4;k++){ t[2*k]+=bflo(r.a[k]); t[2*k+1]+=bfhi(r.a[k]); t[8+2*k]+=bflo(r.b[k]); t[9+2*k]+=bfhi(r.b[k]); }
  #pragma unroll
  for(int k=0;k<2;k++){ t[16+2*k]+=bflo(r.c[k]); t[17+2*k]+=bfhi(r.c[k]); }
}
#define YSM 1
#define YS0 1
#define YSN 4
#define T_XV 10
#define T_YF 4
#define T_PY 7
#define T_CY 9

DI void p2_mfma(const Prm& p, LAS char* lds, int q, int pm, int pn, unsigned w, unsigned lane, u64 tag TSP){
  const int tile=pm*4+pn;
  const srd_t sY=mksrd(p.ws+WS_YX);
  const unsigned wr=w>>1, wc=w&1, g=lane>>4, c16=lane&15;
  const unsigned sbase=((tile*4+w)*4+q)*4*2560, rbase=(tile*4+q)*16*2560+w*2560;
  const unsigned pY=WS_YF+((tile*4+q)*4+(lane&3))*8;
  u64 pv=0;
  Qd yq[4];
  { f32x4 acc[4][5];
    const unsigned pH= lane<32? WS_HF+(pm*16+(lane&15))*8: WS_ABF+((2*pn+(q&1))*32+(lane&31))*8;
    y_loop(lds,w,lane,acc,[&](auto T,auto M){ constexpr int t=T.value, m=M.value;
      if constexpr(t==T_PH && m==17){ if(w==1) poll_issue(pv,p.ws,pH); }
      if constexpr(t==NY-4 && m==-1){ if(w==1){ wait_vm<0>(); poll_check(pv,p.ws,pH,tag); } } });
    TS(7); TSR(18);
    yq[0]=pack_part<0>(acc); yq[1]=pack_part<1>(acc); yq[2]=pack_part<2>(acc); yq[3]=pack_part<3>(acc); }
  const GAddr ad=gate_addr(w,lane); GFrag fr[2];
  { s16x4 tl[3];
    #pragma unroll
    for(int m=0;m<16;m++) gate_rd(m,lds,ad,NY,0,fr[0],tl); }
  GAP();
  auto ysend=[&](auto K,auto P){ constexpr int k=K.value, pt=P.value;
    if((int)w==q){ LAS char* d=lds+YOWN+k*2560;
      if constexpr(pt==0) *(LAS u32x4*)(d+lane*16)=yq[k].a; else if constexpr(pt==1) *(LAS u32x4*)(d+1024+lane*16)=yq[k].b; else *(LAS u32x2*)(d+2048+lane*8)=yq[k].c;
    } else { const unsigned b=sbase+k*2560;
      if constexpr(pt==0) __builtin_amdgcn_raw_buffer_store_b128(yq[k].a,sY,b+lane*16,0,16);
      else if constexpr(pt==1) __builtin_amdgcn_raw_buffer_store_b128(yq[k].b,sY,b+1024+lane*16,0,16);
      else __builtin_amdgcn_raw_buffer_store_b64(yq[k].c,sY,b+2048+lane*8,0,16); } };
  if constexpr(YSM==0) sfor<12>([&](auto N){ ysend(ic<N.value/3>{},ic<N.value%3>{}); });
  else {
    #pragma unroll
    for(int k=0;k<4;k++) asm volatile("":"+v"(yq[k].a),"+v"(yq[k].b),"+v"(yq[k].c)); }
  GAP();
  TS(8);
  f32x4 og[5]; Qd y[3]; u32x2 xv[5];
  const unsigned row0=pm*128+(q>>1)*64+32*wr+c16+16*wc, row1=row0+16-32*wc, col0=pn*160+(q&1)*80+4*g;
  const unsigned oo[5]={row0*D_+col0+(wc? 64: 0), row0*D_+col0+(wc? 48: 16), row1*D_+col0+(wc? 64: 0), row1*D_+col0+(wc? 48: 16), row0*D_+col0+32};
  auto fetch=[&](auto N){ constexpr int n=N.value;
    if constexpr(n<9){ constexpr int k=n/3, pt=n%3; const unsigned b=rbase+((q+1+k)&3)*4*2560;
      if constexpr(pt==0) y[k].a=__builtin_amdgcn_raw_buffer_load_b128(sY,b+lane*16,0,16);
      else if constexpr(pt==1) y[k].b=__builtin_amdgcn_raw_buffer_load_b128(sY,b+1024+lane*16,0,16);
      else y[k].c=__builtin_amdgcn_raw_buffer_load_b64(sY,b+2048+lane*8,0,16); }
    else if constexpr(n<14) xv[n-9]=*(const u32x2*)(p.x+oo[n-9]); };
  gate_loop(lds,ad,fr,og,[&](auto T,auto M){ constexpr int t=T.value, m=M.value;
    if constexpr(YSM==1 && m>=0){ constexpr int n=(2*t+m/10-YS0)*YSN+(m%10)/2;
      if constexpr(2*t+m/10>=YS0 && (m&1)==1 && (m%10)/2<YSN && n<12) ysend(ic<n/3>{},ic<n%3>{}); }
    if constexpr(t==T_YF && m==-1){ wait_vm<0>(); if(lane==0) st_flag(p.ws,WS_YF+((tile*4+w)*4+q)*8,tag); }
    if constexpr(t==T_PY && m==8) poll_issue(pv,p.ws,pY);
    if constexpr(t==T_XV && m>=11 && m<16) fetch(ic<9+m-11>{});
    if constexpr(t==T_CY && m==-1){ wait_vm<0>(); poll_check(pv,p.ws,pY,tag); }
    if constexpr(m>=0){ constexpr int hh=2*(t-T_CY)+m/10-1;
      if constexpr(hh>=0 && hh<3 && (m%10)%3==1 && (m%10)<9) fetch(ic<3*hh+(m%10)/3>{}); } });
  TS(9); TSR(19);
  { const LAS char* d=lds+YOWN+w*2560; Qd y0; y0.a=*(const LAS u32x4*)(d+lane*16); y0.b=*(const LAS u32x4*)(d+1024+lane*16); y0.c=*(const LAS u32x2*)(d+2048+lane*8);
    float ty[20];
    #pragma unroll
    for(int k=0;k<20;k++) ty[k]=0.f;
    add_part(ty,y0);
    #pragma unroll
    for(int k=0;k<3;k++) add_part(ty,y[k]);
    TS(10);
    #pragma unroll
    for(int k=0;k<5;k++){ u32x2 o;
      #pragma unroll
      for(int e=0;e<2;e++){ const float o0=og[k][2*e], o1=og[k][2*e+1];
        o[e]=pk(bflo(xv[k][e])+ty[4*k+2*e]*(o0*sigm(o0)), bfhi(xv[k][e])+ty[4*k+2*e+1]*(o1*sigm(o1))); }
      *(u32x2*)(p.out+oo[k])=o; }
  }
  TS(11);
}
__global__ __launch_bounds__(512,2) void fnet_mlp_fused(Prm p){
  extern __shared__ __attribute__((aligned(1024))) char smem[];
  LAS char* lds=(LAS char*)smem;
  const unsigned tid=threadIdx.x, lane=tid&63, w=rfl(tid>>6);
  const int b=blockIdx.x, x8=b&7, r=b>>3;
  const int q=x8>>1, pm=(x8&1)*8+(r>>2), pn=r&3, j=q*4+pn;
  unsigned tsv=0, tsw=0, tsx=0, tsb=0;
  TSR(14); TS(0);
  if(w>=4){
    const unsigned lw=w-4;
    const u64 tag= lw<2? loader<3>(p,lds,pm,j,q,pn,lw,lane TSA) : loader<2>(p,lds,pm,j,q,pn,lw,lane TSA);
    TS(8);
    if(b==0 && lw==0){
      #pragma unroll 1
      for(int i=0;i<4;i++) wait_flags(p.ws,WS_ABF+(i*64+lane)*8,tag);
      if(lane==0) st_flag(p.ws,0,tag);
    }
  } else {
    const u64 tag=p1_mfma(p,lds,pm,j,w,lane TSA);
    p2_mfma(p,lds,q,pm,pn,w,lane,tag TSA);
  }
  TSR(15); TSPUT();
}

__global__ void dbg_pre(char* ws){ if(threadIdx.x==0){ unsigned t=(unsigned)__builtin_amdgcn_s_memrealtime(); *(volatile unsigned*)(ws+(199u<<20))=t; } }
__global__ void dbg_post(char* ws){
  unsigned now=(unsigned)__builtin_amdgcn_s_memrealtime();
  const unsigned* t=(const unsigned*)(ws+(200u<<20));
  __shared__ int red[8][3][256];
  const int b=threadIdx.x; const unsigned r0=t[14];
  const int L[8]={14,20,16,17,18,19,15,15};
  for(int k=0;k<8;k++){ int mn=1<<30,mx=-(1<<30);
    for(int w=0;w<8;w++){ if(k!=0 && k!=7 && w!=0) continue; int v=(int)(t[(b*8+w)*256+L[k]]-r0); mn=min(mn,v); mx=max(mx,v); }
    red[k][0][b]=mn; red[k][1][b]=mx; red[k][2][b]=mn; }
  __syncthreads();
  if(b==0){ int mn[8],mx[8],av[8];
    for(int k=0;k<8;k++){ mn[k]=1<<30; mx[k]=-(1<<30); av[k]=0; for(int i=0;i<256;i++){ mn[k]=min(mn[k],red[k][0][i]); mx[k]=max(mx[k],red[k][1][i]); av[k]+=red[k][2][i]; } av[k]/=256; }
    const int z=mn[0]; const int pre=(int)(*(const unsigned*)(ws+(199u<<20))-r0);
    printf("DBG pre->entry %d entrymax %d start1 %d/%d/%d abepi %d start2 %d/%d/%d yend %d/%d/%d gend %d/%d/%d done %d span %d post %d\n", z-pre, mx[0]-z, mn[1]-z,av[1]-z,mx[1]-z, av[2]-z, mn[3]-z,av[3]-z,mx[3]-z, mn[4]-z,av[4]-z,mx[4]-z, mn[5]-z,av[5]-z,mx[5]-z, av[6]-z, mx[7]-z, (int)(now-r0)-mx[7]);
  }
}
extern "C" void kernel_launch(void* const* d_in, const int* in_sizes, int n_in, void* d_out, int out_size, void* d_ws, size_t ws_size, hipStream_t stream) {
  Prm p; p.x=(const u16*)d_in[0]; p.wsre=(const u16*)d_in[1]; p.wsim=(const u16*)d_in[2]; p.wdre=(const u16*)d_in[3]; p.wdim=(const u16*)d_in[4];
  p.w1=(const u16*)d_in[6]; p.w2=(const u16*)d_in[7]; p.out=(u16*)d_out; p.ws=(char*)d_ws+WSOFF;
  (void)hipFuncSetAttribute((const void*)fnet_mlp_fused, hipFuncAttributeMaxDynamicSharedMemorySize, LDS_BYTES);
  dbg_pre<<<1,64,0,stream>>>((char*)d_ws);
  fnet_mlp_fused<<<256,512,LDS_BYTES,stream>>>(p);
  dbg_post<<<1,256,0,stream>>>((char*)d_ws);
}
```

```cpp
#include <hip/hip_runtime.h>
#include <stdint.h>
#include <type_traits>
#include <utility>
typedef unsigned short u16;
typedef unsigned long long u64;
typedef float f32x4 __attribute__((ext_vector_type(4)));
typedef float f32x2 __attribute__((ext_vector_type(2)));
typedef __bf16 bf16x8 __attribute__((ext_vector_type(8)));
typedef __bf16 bf16x2 __attribute__((ext_vector_type(2)));
typedef short s16x4 __attribute__((ext_vector_type(4)));
typedef short s16x8 __attribute__((ext_vector_type(8)));
typedef unsigned u32x4 __attribute__((ext_vector_type(4)));
typedef unsigned u32x2 __attribute__((ext_vector_type(2)));
#define DI __device__ __forceinline__
#define GAP() __builtin_amdgcn_sched_barrier(0)
#define LAS __attribute__((address_space(3)))
#define GAS __attribute__((address_space(1)))
typedef GAS u64 gu64;
typedef GAS unsigned gu32;
typedef __amdgpu_buffer_rsrc_t srd_t;

constexpr int S_=2048, D_=640, F_=1536;
constexpr unsigned WS_ABF=8192, WS_HF=12288, WS_YF=16384, WS_RST=65536;
constexpr unsigned WS_ABT=1u<<20, WS_H=8u<<20, WS_YX=16u<<20, WS_W2=30u<<20;
constexpr int XS(int i){ return i*16384; }
constexpr int WS_(int i){ return 98304+i*12288; }
constexpr int ABS1=XS(4), ABS2=155648, AUX=159744, STG=AUX, ROWM=AUX+2048, ROWR=ROWM+512, CSX=ROWM+1024, LDS_BYTES=163840;
constexpr int xab(int i){ return XS(i%6); }
constexpr int wab(int i){ return WS_(i%5); }
constexpr int xh(int n){ return n<5? XS((5+n)%6): n==5? XS(5): XS(n-6); }
constexpr int wh(int n){ constexpr int m[10]={0,1,2,3,0,4,1,2,3,4}; return WS_(m[n]); }
constexpr int PA[5]={XS(4),XS(0),XS(1),XS(2),XS(3)}, PB[4]={81920,102400,122880,143360};
constexpr int HST0=PA[4], HST1=PB[3], YOWN=PA[0];
constexpr unsigned SPIN_MAX=40000;
#define WSOFF 0
#define ROTEXPR ((pn+pm)&3)
#define YG 4
#define OROT rot
#define YOFF ((pm+q)&3)
#define OOFF ((pm+q)&3)
#define K0EXPR ((pm+3*j)%10)
constexpr int NY=16, NO=12, NT=NY+NO;
constexpr int slotA(int t){ return t<NY? PA[t%5]: PA[1+(t-NY)%4]; }
constexpr int slotB(int t){ return PB[t&3]; }

#define TS(i)
#define TB(t)
#define TSR(l)
#define TSPUT()
#define TW(t)
#define TX(t)
#define TSP
#define TSA

template<int I> using ic=std::integral_constant<int,I>;
template<int... I,class F> DI void sfor_(std::integer_sequence<int,I...>, F&& f){ (f(ic<I>{}),...); }
template<int N,class F> DI void sfor(F&& f){ sfor_(std::make_integer_sequence<int,N>{},f); }
DI unsigned rfl(unsigned x){ return __builtin_amdgcn_readfirstlane(x); }
DI float bflo(unsigned v){ return __uint_as_float(v<<16); }
DI float bfhi(unsigned v){ return __uint_as_float(v&0xffff0000u); }
DI unsigned pk(float lo,float hi){ f32x2 v={lo,hi}; bf16x2 b=__builtin_convertvector(v,bf16x2); return __builtin_bit_cast(unsigned,b); }
DI float sigm(float v){ return __builtin_amdgcn_rcpf(1.f+__builtin_amdgcn_exp2f(-1.4426950408889634f*v)); }
template<int N> DI void wait_vm(){ asm volatile("s_waitcnt vmcnt(%0)"::"n"(N):"memory"); }
DI void bar(){ __builtin_amdgcn_s_barrier(); }
DI void wait_lgkm(){ __builtin_amdgcn_s_waitcnt(0xC07F); }
constexpr unsigned BIAS=4096;
#define DMA1(O,X) "buffer_load_dwordx4 %" #O ", %1, %2 offen" X " lds"
#define DMA_GROUP(X) \
  if constexpr(N==1) asm volatile("s_mov_b32 m0, %0\n\ts_nop 0\n\tbuffer_load_dwordx4 %3, %1, %2 offen" X " lds" :: "s"(m0v),"s"(srd),"s"(soff),"v"(v[0]) : "memory"); \
  if constexpr(N==2) asm volatile("s_mov_b32 m0, %0\n\ts_nop 0\n\tbuffer_load_dwordx4 %3, %1, %2 offen" X " lds\n\tbuffer_load_dwordx4 %4, %1, %2 offen offset:1024" X " lds" :: "s"(m0v),"s"(srd),"s"(soff),"v"(v[0]),"v"(v[1]) : "memory"); \
  if constexpr(N==3) asm volatile("s_mov_b32 m0, %0\n\ts_nop 0\n\tbuffer_load_dwordx4 %3, %1, %2 offen" X " lds\n\tbuffer_load_dwordx4 %4, %1, %2 offen offset:1024" X " lds\n\tbuffer_load_dwordx4 %5, %1, %2 offen offset:2048" X " lds" :: "s"(m0v),"s"(srd),"s"(soff),"v"(v[0]),"v"(v[1]),"v"(v[2]) : "memory"); \
  if constexpr(N==4) asm volatile("s_mov_b32 m0, %0\n\ts_nop 0\n\tbuffer_load_dwordx4 %3, %1, %2 offen" X " lds\n\tbuffer_load_dwordx4 %4, %1, %2 offen offset:1024" X " lds\n\tbuffer_load_dwordx4 %5, %1, %2 offen offset:2048" X " lds\n\tbuffer_load_dwordx4 %6, %1, %2 offen offset:3072" X " lds" :: "s"(m0v),"s"(srd),"s"(soff),"v"(v[0]),"v"(v[1]),"v"(v[2]),"v"(v[3]) : "memory");
#define SC1S " sc1"
template<int N,int POL> DI void dma_group(const __amdgpu_buffer_rsrc_t& srd, const unsigned* v, unsigned soff, unsigned m0v){
  if constexpr(POL==1){ DMA_GROUP(SC1S) } else if constexpr(POL==2){ DMA_GROUP(" nt") } else { DMA_GROUP("") }
}
template<int POL> DI void dma5(const __amdgpu_buffer_rsrc_t& srd, const unsigned* v, unsigned soff, unsigned m0v){
  dma_group<4,POL>(srd,v,soff,m0v); dma_group<1,POL>(srd,v+4,soff,m0v+4096u);
}
#define NTX 0
#define NTD 0
#define NT1 0
#define NTS 0
#define NTSF NTS
#define NT2 0
DI srd_t mksrd(const void* p){ return __builtin_amdgcn_make_buffer_rsrc((void*)p,0,0x7ffffff0,0x00020000); }
DI srd_t mksrd_lo(const void* p){ return mksrd((const char*)p-BIAS); }
DI void st_flag(char* ws, unsigned off, u64 tag){ __hip_atomic_store((gu64*)(ws+off), tag, __ATOMIC_RELAXED, __HIP_MEMORY_SCOPE_AGENT); }
DI void st_f32(char* ws, unsigned off, float v){ __hip_atomic_store((gu32*)(ws+off), __float_as_uint(v), __ATOMIC_RELAXED, __HIP_MEMORY_SCOPE_AGENT); }
DI u64 ld_flag(char* ws, unsigned off){ return __hip_atomic_load((gu64*)(ws+off), __ATOMIC_RELAXED, __HIP_MEMORY_SCOPE_AGENT); }
DI void wait_flags(char* ws, unsigned off, u64 tag){
  #pragma unroll 1
  for(unsigned s=0;s<SPIN_MAX;s++){ const u64 v=ld_flag(ws,off); if(__all(v==tag)) break; __builtin_amdgcn_s_sleep(2); }
}
DI u64 get_tag(const char* ws){ u64 ep; asm volatile("s_load_dwordx2 %0, %1, 0x0\n\ts_waitcnt lgkmcnt(0)":"=s"(ep):"s"(ws):"memory"); return ep+1; }
DI void poll_issue(u64& v, char* ws, unsigned off){ const char* a=ws+off; asm volatile("global_load_dwordx2 %0, %1, off sc1":"=v"(v):"v"(a):"memory"); }
DI void poll_check(u64& v, char* ws, unsigned off, u64 tag){ asm volatile("":"+v"(v)); if(!__all(v==tag)) wait_flags(ws,off,tag); }
DI const LAS char* at(unsigned reg,int off){ return (const LAS char*)(size_t)reg+off; }
#define PIN(x) asm volatile("":"+v"(x))
DI bf16x8 ldf(const LAS char* a){ return *(const LAS bf16x8*)a; }
DI bf16x8 ldtr(const LAS char* a, int step){
  const s16x4 lo=__builtin_amdgcn_ds_read_tr16_b64_v4i16((LAS s16x4*)a), hi=__builtin_amdgcn_ds_read_tr16_b64_v4i16((LAS s16x4*)(a+step));
  return __builtin_bit_cast(bf16x8,(s16x8)__builtin_shufflevector(lo,hi,0,1,2,3,4,5,6,7));
}
DI s16x4 ldtr1(const LAS char* a){ return __builtin_amdgcn_ds_read_tr16_b64_v4i16((LAS s16x4*)a); }
DI bf16x8 cat(s16x4 lo, s16x4 hi){ return __builtin_bit_cast(bf16x8,(s16x8)__builtin_shufflevector(lo,hi,0,1,2,3,4,5,6,7)); }
DI f32x4 mma(bf16x8 a, bf16x8 b, f32x4 c){ return __builtin_amdgcn_mfma_f32_16x16x32_bf16(a,b,c,0,0,0); }

template<int CTRL> DI float dpp_add(float v){ return v+__int_as_float(__builtin_amdgcn_update_dpp(0,__float_as_int(v),CTRL,0xf,0xf,true)); }
DI float sum16(float v){ v=dpp_add<0xB1>(v); v=dpp_add<0x4E>(v); v=dpp_add<0x141>(v); return dpp_add<0x140>(v); }

struct Prm { const u16* x; const u16* wsre; const u16* wsim; const u16* wdre; const u16* wdim; const u16* w1; const u16* w2; u16* out; char* ws; };

enum { K_X, K_WD, K_XR, K_W1, K_YA, K_YB, K_OA, K_OB, K_STAT, K_STCHK, K_ABS, K_FAB, K_PAB, K_CAB, K_HRD, K_HS, K_FH, K_PF, K_W2L, K_W2S };
struct Op { int k, i, step; };
struct Sched { Op op[200]; int n; };
constexpr int TB1=20, TP2=21, TEND=TP2+NT;
#define ST_FAB 15
#define ST_PAB 18
#define ST_YA0 16
#define ST_YA1 17
#define ST_YA2 19
#define ST_YA3 19
#define PFU 4
#define PFY 0
#define ST_PFY 9
#define ST_PFW 100
#define ST_PF1 100
#define ST_W2L 6
#define ST_W2S 9
#define ST_FH 8
#define T_PH 10
#define YPAIR 0
constexpr Sched make_sched(){
  Sched s{}; auto add=[&](int k,int i,int st){ s.op[s.n].k=k; s.op[s.n].i=i; s.op[s.n].step=st; s.n++; };
#define PRO 1
  if(PRO==0){ add(K_X,0,-2); add(K_WD,0,-2); add(K_X,1,-2); add(K_WD,1,-2); add(K_X,2,-1); add(K_WD,2,-1); add(K_X,3,-1); }
  if(PRO==1){ add(K_X,0,-2); add(K_WD,0,-2); add(K_X,1,-2); add(K_X,2,-2); add(K_WD,1,-2); add(K_WD,2,-1); add(K_X,3,-1); }
  if(PRO==2){ add(K_X,0,-2); add(K_WD,0,-2); add(K_X,1,-2); add(K_WD,1,-2); add(K_WD,2,-2); add(K_X,2,-1); add(K_X,3,-1); }
  for(int st=0;st<20;st++){
    if(st==4) add(K_STAT,0,st);
    if(st==ST_FAB) add(K_FAB,0,st);
    if(st==ST_PAB) add(K_PAB,0,st);
    if(st<=6) add(K_WD,st+3,st);
    if(st<=5) add(K_X,st+4,st);
    if(st>=5 && st<=8) add(K_W1,st-5,st);
    if(st==ST_W2L) add(K_W2L,0,st);
    if(st==8) add(K_STCHK,0,st);
    if(st==ST_W2S) add(K_W2S,0,st);
    if(st==ST_PFY && PFY>0) add(K_PF,0,st);
    if(st==ST_PFY && PFY>1) add(K_PF,1,st);
    if(st==ST_PFW) add(K_PF,2,st);
    if(st==ST_PF1) add(K_PF,3,st);
    if(st==10){ add(K_W1,4,st); add(K_ABS,0,st); }
    if(st==11){ add(K_XR,0,st); add(K_W1,5,st); }
    if(st>=12 && st<=15){ add(K_XR,st-11,st); add(K_W1,st-6,st); }
    if(st==ST_YA0) add(K_YA,0,st);
    if(st==ST_YA1) add(K_YA,1,st);
    if(st==19){ add(K_CAB,0,st); add(K_YB,0,st); add(K_YB,1,st);
      if(ST_YA2==19) add(K_YA,2,st); add(K_YB,2,st); if(ST_YA3==19) add(K_YA,3,st); }
    if(st==ST_YA2 && st!=19) add(K_YA,2,st);
    if(st==ST_YA3 && st!=19) add(K_YA,3,st);
  }
  add(K_HRD,0,TB1);
  for(int t=0;t<NT;t++){ const int st=TP2+t;
    if(t==ST_FH) add(K_FH,0,st);
    if(t==0){ add(K_YB,3,st); if(YPAIR) add(K_YA,5-1,st); else add(K_YA,4,st); }
    if(t<6 && !(YPAIR && t==0)) add(K_HS,t,st);
    if(YPAIR==0){
      if(t+4<NY) add(K_YB,t+4,st);
      if(t+5<NY) add(K_YA,t+5,st); }
    if(YPAIR==1){
      if(t+4<NY) add(K_YB,t+4,st);
      if(t==0) add(K_YA,5,st);
      if(t>0 && (t&1)==0){ if(t+4<NY) add(K_YA,t+4,st); if(t+5<NY) add(K_YA,t+5,st); } }
    if(YPAIR==2){
      if(t==0) add(K_YA,5,st);
      if(t>0 && (t&1)==0){ if(t+4<NY) add(K_YA,t+4,st); if(t+5<NY) add(K_YA,t+5,st); }
      if(t+4<NY) add(K_YB,t+4,st); }
    if(YPAIR && t==0) add(K_HS,0,st);
    if(t+4>=NY && t+4<NT){ add(K_OA,t+4,st); add(K_OB,t+4,st); }
  }
  return s;
}
constexpr Sched SCH=make_sched();
constexpr int op_cnt(const Op& o,int nwd){
  return o.k==K_WD? nwd: o.k==K_W1? 3: (o.k==K_X||o.k==K_XR||o.k==K_YA||o.k==K_OA)? 4: (o.k==K_YB||o.k==K_OB||o.k==K_ABS)? 5: (o.k==K_W2L||o.k==K_W2S)? 2: (o.k==K_HS||o.k==K_PAB||(o.k==K_PF && o.i<2))? 1: 0; }
constexpr int first_use(const Op& o){
  return (o.k==K_X||o.k==K_WD)? o.i-1: o.k==K_W1? o.i+9: o.k==K_XR? o.i+14: (o.k==K_YA||o.k==K_YB||o.k==K_OA||o.k==K_OB)? TB1+o.i: o.k==K_STAT? 8: 1000; }
constexpr int last_use(const Op& o){
  return o.k==K_X? (o.i>=5? o.i+5: o.i): o.k==K_WD? o.i: o.k==K_W1? o.i+10: o.k==K_XR? o.i+15: (o.k==K_YA||o.k==K_YB||o.k==K_OA||o.k==K_OB)? TP2+o.i: o.k==K_STAT? 9: -1000; }
constexpr int lds_lo(const Op& o){
  return o.k==K_X? xab(o.i): o.k==K_WD? wab(o.i): o.k==K_W1? wh(o.i): o.k==K_XR? xh(o.i+5): (o.k==K_YA||o.k==K_OA)? slotA(o.i): (o.k==K_YB||o.k==K_OB)? slotB(o.i): o.k==K_STAT? STG: -1; }
constexpr int lds_sz(const Op& o){
  return o.k==K_WD? 10240: o.k==K_W1? 12288: (o.k==K_X||o.k==K_XR||o.k==K_YA||o.k==K_OA)? 16384: (o.k==K_YB||o.k==K_OB)? 20480: o.k==K_STAT? 2048: 0; }
constexpr bool needed(const Op& o,int tau){ return first_use(o)==tau; }
constexpr int last_needed(int tau){ int l=-1; for(int n=0;n<SCH.n;n++) if(SCH.op[n].step<tau && needed(SCH.op[n],tau)) l=n; return l; }
constexpr int allowed(int tau,int nwd){ const int l=last_needed(tau); if(l<0) return -1; int c=0; for(int n=l+1;n<SCH.n;n++) if(SCH.op[n].step<tau) c+=op_cnt(SCH.op[n],nwd); return c; }
constexpr int find_op(int k,int i){ for(int n=0;n<SCH.n;n++) if(SCH.op[n].k==k && SCH.op[n].i==i) return n; return 1<<20; }
constexpr bool done_by(int k,int i,int tau){ const int p=find_op(k,i); for(int t=-1;t<=tau;t++) if(last_needed(t)>=p) return true; return false; }
constexpr int behind(int k0,int k1,int nwd){ int c=0; for(int n=find_op(k0,0)+1;n<find_op(k1,0);n++) c+=op_cnt(SCH.op[n],nwd); return c; }
constexpr int step_begin(int st){ int n=0; while(n<SCH.n && SCH.op[n].step<st) n++; return n; }
struct Reg { int lo, sz, from, to; };
constexpr Reg STAGED[7]={ {ABS1,16384,9,11}, {ABS2,4096,9,11}, {HST0,16384,19,TP2}, {HST1,16384,19,TP2}, {YOWN,10240,TP2+NY-1,1000},
                          {ROWM,1024,8,TB1}, {CSX,1024,7,TB1} };
constexpr bool sched_ok(){
  for(int n=0;n<SCH.n;n++){ const Op& a=SCH.op[n];
    if(n && a.step<SCH.op[n-1].step) return false;
    if(lds_sz(a)==0) continue;
    if(a.step>=first_use(a)) return false;
    for(int m=0;m<SCH.n;m++){ const Op& b=SCH.op[m]; if(m==n || lds_sz(b)==0) continue;
      if(lds_lo(a)<lds_lo(b)+lds_sz(b) && lds_lo(b)<lds_lo(a)+lds_sz(a) && a.step<last_use(b) && b.step<last_use(a)) return false; }
    for(const Reg& r: STAGED) if(lds_lo(a)<r.lo+r.sz && r.lo<lds_lo(a)+lds_sz(a) && a.step<r.to && r.from<last_use(a)) return false; }
  for(int k: {K_X,K_WD,K_W1}) for(int i=0;i<10;i++) if(find_op(k,i)>=SCH.n) return false;
  for(int i=0;i<5;i++) if(find_op(K_XR,i)>=SCH.n) return false;
  for(int i=0;i<NT;i++) if(find_op(i<NY? K_YA: K_OA,i)>=SCH.n || find_op(i<NY? K_YB: K_OB,i)>=SCH.n) return false;
  return true; }
static_assert(sched_ok(),"LDS regions are reused only behind the barrier that ends their last read");
static_assert(find_op(K_W2L,0)<find_op(K_W2S,0) && find_op(K_W2S,0)<find_op(K_ABS,0),"the a/b flag also covers the re-laid part of w2");
static_assert(done_by(K_ABS,0,ST_FAB) && done_by(K_HS,5,TP2+ST_FH),"a flag follows its completed stores");
static_assert(find_op(K_CAB,0)<find_op(K_YB,0) && find_op(K_PAB,0)<find_op(K_CAB,0),"a/b tiles are loaded behind the check of their flags");
static_assert(T_PH<NY-4 && SCH.op[find_op(K_OA,NY)].step>=TP2+NY-4,"h tiles are loaded behind the check of their flags");

template<int NWD>
DI u64 loader(const Prm& p, LAS char* lds, int pm, int j, int q, int pn, unsigned lw, unsigned lane TSP){
  const unsigned ldsb=(unsigned)(size_t)lds;
  const unsigned r=lane>>3;
  const int rot=ROTEXPR, k0=K0EXPR, orot=OROT, yoff=YOFF, ooff=OOFF;
  u64 tag, pv=0;
  const srd_t sA=mksrd_lo(p.x+(size_t)pm*128*D_), sD=mksrd_lo((j<8? p.wdre: p.wdim)+(size_t)(j&7)*80*D_), sW=mksrd_lo(p.w1+j*96);
  const unsigned q0= NWD==3? 3*lw: 2*lw+2;
  unsigned vX[4], vD[NWD], vW[3];
  #pragma unroll
  for(int i=0;i<4;i++) vX[i]=(32*lw+16*(i&1)+4*(i>>1)+8*(r>>2)+(r&3))*(D_*2)+((lane&7)^((i&1)*4+(r>>1)))*16+BIAS-1024*i;
  #pragma unroll
  for(int i=0;i<NWD;i++) vD[i]=((q0+i)*8+r)*(D_*2)+((lane&7)^(((q0+i)&1)*4+(r>>1)))*16+BIAS-1024*i;
  #pragma unroll
  for(int i=0;i<3;i++){ const unsigned c=i*64+lane, row=c/12, ch=c%12; vW[i]=(16*lw+row)*(F_*2)+32*((ch>>1)^((row>>3)&1))+16*(ch&1)+BIAS-1024*i; }
  auto ktile=[&](int i){ const int kt=k0+i; return kt>=10? kt-10: kt; };
  auto ldx=[&](int i,int dst){ dma_group<4,NTX>(sA,vX,ktile(i)*128u,ldsb+lw*4096u+dst); };
  auto ldwd=[&](int i){ dma_group<NWD,NTD>(sD,vD,ktile(i)*128u,ldsb+q0*1024u+wab(i)); };
  constexpr int NPRE=step_begin(-1);
  TS(1);
  sfor<NPRE>([&](auto N){ constexpr Op o=SCH.op[N.value]; static_assert(o.k==K_X||o.k==K_WD,"prologue");
    if constexpr(o.k==K_X) ldx(o.i,xab(o.i)); else ldwd(o.i); });
  TS(2);
  tag=get_tag(p.ws);
  const srd_t sWs=mksrd_lo((q<2? p.wsre: p.wsim)+(size_t)pm*128*S_+(q&1)*1024), sT=mksrd_lo(p.ws+WS_ABT+(size_t)pn*160*8192+q*2048);
  const srd_t sH=mksrd_lo(p.ws+WS_H+(size_t)pm*48*8192+(q>>1)*4096), sW2=mksrd_lo(p.ws+WS_W2+(2*pn+(q&1))*245760), sWk=mksrd(p.ws);
  unsigned vA[4], vB[5], vH[4], vW2[5];
  #pragma unroll
  for(int i=0;i<4;i++){ const unsigned c=(lane&7)^((i&1)*4+(r>>1));
    vA[i]=(32*lw+8*i+r)*(S_*2)+c*16+BIAS-1024*i;
    vH[i]=(2*(lw>>1)+(c>>2))*8192+(32*(lw&1)+8*i+r)*64+(c&3)*16+BIAS-1024*i; }
  #pragma unroll
  for(int i=0;i<5;i++){ const unsigned bi=BIAS-(i<4? 1024*i: 0);
    vB[i]=((5*lw+i)*8+r)*8192+((lane&7)^(((lw+i)&1)*4+(r>>1)))*16+bi;
    vW2[i]=lw*5120+lane*16+BIAS+(i<4? 0: 4096); }
  u32x4 wv[2]; unsigned wsrc[2], wdst[2];
  #pragma unroll
  for(int i=0;i<2;i++){ const unsigned cc=i*60+(lane<60? lane: 59), rho=(2*pm+(j&1))*48+12*lw+cc/10, ch=cc%10, row=rho^(((rho>>3)&1)<<2), sl=j>>1;
    wsrc[i]=row*(D_*2)+((sl>>1)*160+(sl&1)*80)*2+ch*16; wdst[i]=WS_W2+sl*245760+rho*160+ch*16; }
  const srd_t sW2s=mksrd(p.w2);
  const unsigned lA2=ldsb+lw*4096u, lB2=ldsb+lw*5120u;
  u32x4 hv[6]; unsigned pfv=0;
  const unsigned pAB=WS_ABF+((((q>>1)*4+pn)*16+(q&1)*8)*2+(lane&15))*8;
  auto exec=[&](auto N){ constexpr Op o=SCH.op[N.value];
    if constexpr(o.k==K_X) ldx(o.i,xab(o.i));
    if constexpr(o.k==K_XR) ldx(o.i,xh(o.i+5));
    if constexpr(o.k==K_WD) ldwd(o.i);
    if constexpr(o.k==K_W1) dma_group<3,NT1>(sW,vW,ktile(o.i>=5? o.i-5: o.i+5)*(64u*F_*2),ldsb+lw*3072u+wh(o.i));
    if constexpr(o.k==K_YA){ const int i2=(o.i+yoff)&15, u=(i2&~(YG-1))|((i2+rot)&(YG-1));
      if constexpr(NTSF!=NTS){ if(o.i<=(YPAIR?1:0) || (i2&(YG-1))<=(YPAIR?1:0)) dma_group<4,NTSF>(sWs,vA,u*128u,lA2+slotA(o.i)); else dma_group<4,NTS>(sWs,vA,u*128u,lA2+slotA(o.i)); }
      else dma_group<4,NTS>(sWs,vA,u*128u,lA2+slotA(o.i)); }
    if constexpr(o.k==K_YB){ const int i2=(o.i+yoff)&15, u=(i2&~(YG-1))|((i2+rot)&(YG-1)); dma5<1>(sT,vB,u*128u,lB2+slotB(o.i)); }
    if constexpr(o.k==K_OA){ int i2=o.i-NY+ooff; i2= i2>=NO? i2-NO: i2; const int u=(i2&~3)|((i2+orot)&3); dma_group<4,1>(sH,vH,u*32768u,lA2+slotA(o.i)); }
    if constexpr(o.k==K_OB){ int i2=o.i-NY+ooff; i2= i2>=NO? i2-NO: i2; const int u=(i2&~3)|((i2+orot)&3); dma5<1>(sW2,vW2,u*20480u,lB2+slotB(o.i)); }
    if constexpr(o.k==K_STAT){ if(NWD==3 && lw==0){ const unsigned v0=WS_RST+pm*2048+lane*16, vs[2]={v0,v0}; dma_group<2,1>(sWk,vs,0,ldsb+STG); } }
    if constexpr(o.k==K_STCHK){ if(NWD==3 && lw==0){
        u32x4 a=*(const LAS u32x4*)(lds+STG+lane*16), b=*(const LAS u32x4*)(lds+STG+1024+lane*16);
        const unsigned tg=(unsigned)tag;
        if(!__all(a[1]==tg && a[3]==tg && b[1]==tg && b[3]==tg)){
          #pragma unroll 1
          for(unsigned sp=0;sp<SPIN_MAX;sp++){ asm volatile("":::"memory");
            a=__builtin_amdgcn_raw_buffer_load_b128(sWk,WS_RST+pm*2048+lane*16,0,16); b=__builtin_amdgcn_raw_buffer_load_b128(sWk,WS_RST+pm*2048+1024+lane*16,0,16);
            if(__all(a[1]==tg && a[3]==tg && b[1]==tg && b[3]==tg)) break; } }
        LAS unsigned* rm=(LAS unsigned*)(lds+ROWM); LAS unsigned* rr=(LAS unsigned*)(lds+ROWR);
        rm[lane]=a[0]; rr[lane]=a[2]; rm[64+lane]=b[0]; rr[64+lane]=b[2];
        wait_lgkm(); } }
    if constexpr(o.k==K_ABS){
      u32x4 v[5];
      #pragma unroll
      for(int n=0;n<5;n++){ const unsigned col=20*lw+4*n+(lane>>4); v[n]=*(const LAS u32x4*)(lds+(20*lw+4*n<64? ABS1: ABS2-64*256)+col*256+(((lane&15)^(col&15))*16)); }
      #pragma unroll
      for(int n=0;n<5;n++){ const unsigned col=20*lw+4*n+(lane>>4);
        __builtin_amdgcn_raw_buffer_store_b128(v[n],sWk,WS_ABT+((j&7)*80+col)*8192+(j>>3)*4096+pm*256+(lane&15)*16,0,16); } }
    if constexpr(o.k==K_FAB){ TSR(16); if(lw==0 && lane==0) st_flag(p.ws,WS_ABF+((((j>>3)*4+((j&7)>>1))*16+pm)*2+(j&1))*8,tag); }
    if constexpr(o.k==K_PF){
      const char* a;
      if constexpr(o.i<2) a=(const char*)((q<2? p.wsre: p.wsim)+(size_t)(pm*128+32*pn+8*lw+(lane>>3))*S_+(q&1)*1024+((PFU+8*o.i+(lane&7))&15)*64);
      else if constexpr(o.i==2) a=(const char*)p.w2+((size_t)blockIdx.x*64+lane)*120;
      else a=(const char*)p.w1+((size_t)blockIdx.x*64+lane)*120;
      if(o.i<2 || lw==3) asm volatile("global_load_dword %0, %1, off":"+v"(pfv):"v"(a):"memory"); }
    if constexpr(o.k==K_W2L){
      #pragma unroll
      for(int i=0;i<2;i++) asm volatile("buffer_load_dwordx4 %0, %1, %2, 0 offen":"=v"(wv[i]):"v"(wsrc[i]),"s"(sW2s):"memory"); }
    if constexpr(o.k==K_W2S){ wait_vm<behind(K_W2L,K_W2S,NWD)>(); asm volatile("":"+v"(wv[0]),"+v"(wv[1]));
      #pragma unroll
      for(int i=0;i<2;i++) __builtin_amdgcn_raw_buffer_store_b128(wv[i],sWk,wdst[i],0,16); }
    if constexpr(o.k==K_PAB) poll_issue(pv,p.ws,pAB);
    if constexpr(o.k==K_CAB){ wait_vm<behind(K_PAB,K_CAB,NWD)>(); poll_check(pv,p.ws,pAB,tag); TSR(17); }
    if constexpr(o.k==K_HRD){
      #pragma unroll
      for(int rg=0;rg<2;rg++){ const unsigned s=(lw*2+rg)*16+(lane>>2), sw=((s&3)<<2)^(((s>>1)&1)|(((s>>3)&1)<<1));
        #pragma unroll
        for(int G=0;G<3;G++) hv[rg*3+G]=*(const LAS u32x4*)(lds+(lw<2? HST0: HST1-64*256)+s*256+(((4*G+(lane&3))^sw)*16)); }
      wait_lgkm(); }
    if constexpr(o.k==K_HS) __builtin_amdgcn_raw_buffer_store_b128(hv[o.i],sWk,WS_H+(pm*48+3*j+o.i%3)*8192+((lw*2+o.i/3)*16+(lane>>2))*64+(lane&3)*16,0,16);
    if constexpr(o.k==K_FH){ if(lw==0 && lane==0) st_flag(p.ws,WS_HF+(pm*16+j)*8,tag); }
  };
  sfor<TEND+1>([&](auto I){ constexpr int tau=I.value-1, al=allowed(tau,NWD);
    static_assert(al<64,"vmcnt range");
    TW(tau);
    if constexpr(al>=0) wait_vm<al>();
    TX(tau);
    bar();
    TB(tau);
    constexpr int b0=step_begin(tau), b1=step_begin(tau+1);
    sfor<b1-b0>([&](auto M){ exec(ic<b0+M.value>{}); });
  });
  asm volatile(""::"v"(pfv));
  return tag;
}
DI u64 p1_mfma(const Prm& p, LAS char* lds, int pm, int j, unsigned w, unsigned lane TSP){
  u64 tag;
  const unsigned wr=w>>1, wc=w&1, g=lane>>4, c16=lane&15;
  if(w<2){ const unsigned row=pm*128+j*8+w*4+g; u32x4 v[5]; float s=0;
    #pragma unroll
    for(int i=0;i<5;i++) v[i]=*(const u32x4*)(p.x+(size_t)row*D_+(c16+16*i)*8);
    tag=get_tag(p.ws);
    #pragma unroll
    for(int i=0;i<5;i++)
      #pragma unroll
      for(int k=0;k<4;k++) s+=bflo(v[i][k])+bfhi(v[i][k]);
    const float m=sum16(s)*(1.f/D_); float q2=0;
    #pragma unroll
    for(int i=0;i<5;i++)
      #pragma unroll
      for(int k=0;k<4;k++){ const float a=bflo(v[i][k])-m, c=bfhi(v[i][k])-m; q2+=a*a+c*c; }
    const float rs=1.0f/sqrtf(sum16(q2)*(1.f/D_)+1e-5f);
    if(c16<2) st_flag(p.ws,WS_RST+row*16+c16*8, ((u64)(unsigned)tag<<32)|__float_as_uint(c16==0? m: rs));
  } else tag=get_tag(p.ws);
  TS(1);
  const u32x4 one4={0x3F803F80u,0x3F803F80u,0x3F803F80u,0x3F803F80u}; const bf16x8 ones=__builtin_bit_cast(bf16x8,one4);
  const unsigned rho=3*wr;
  const unsigned lb=(unsigned)(size_t)lds;
  unsigned aA[2][2], aB[2][5], hA[2][2], hB[2][3], aS[5], aC[5], hb[4], hx[4], hs[4];
  #pragma unroll
  for(int s=0;s<2;s++){
    { const unsigned ch=((4*(s^wc)+g)^((lane>>1)&7))*16; aA[0][s]=lb+(32*w+c16)*128+ch; aA[1][s]=aA[0][s]+65536;
      #pragma unroll
      for(int f=0;f<5;f++){ const unsigned fr=(f+rho)%5; aB[s][f]=lb+WS_(0)+(fr*16+c16)*128+ch; } }
    { const unsigned ks=s^wr, ch=((4*ks+g)^((lane>>1)&7))*16; hA[0][s]=lb+(wr*64+c16)*128+ch; hA[1][s]=hA[0][s]+65536;
      #pragma unroll
      for(int f=0;f<3;f++) hB[s][f]=lb+WS_(0)+192*(8*g+(c16>>2))+8*(lane&3)+32*((3*wc+f)^(g&1))+ks*6144; }
  }
  #pragma unroll
  for(int f=0;f<5;f++){ const unsigned fr=(f+rho)%5, col=fr*16+c16;
    aS[f]=lb+(fr<4? ABS1: ABS2-64*256)+col*256+(((4*w+g)^c16)*16); aC[f]=lb+CSX+col*4; }
  #pragma unroll
  for(int fm=0;fm<4;fm++){ const unsigned s=wr*64+32*(fm>>1)+16*(c16>>3)+8*((c16>>2)&1)+4*(fm&1)+(c16&3);
    hs[fm]=lb+ROWM+s*4; hb[fm]=lb+(wr? HST1-64*256: HST0)+s*256+(g&1)*8; hx[fm]=((g>>1)^((s&3)<<2)^(((s>>1)&1)|(((s>>3)&1)<<1)))*16; }
  unsigned aR=lb+ROWM+(32*w+8*g)*4, aH=lb+CSX+(wc*48+4*g)*4;
  #pragma unroll
  for(int s=0;s<2;s++){ PIN(aA[0][s]); PIN(aA[1][s]); PIN(hA[0][s]); PIN(hA[1][s]);
    #pragma unroll
    for(int f=0;f<5;f++) PIN(aB[s][f]);
    #pragma unroll
    for(int f=0;f<3;f++) PIN(hB[s][f]); }
  #pragma unroll
  for(int f=0;f<5;f++){ PIN(aS[f]); PIN(aC[f]); }
  #pragma unroll
  for(int fm=0;fm<4;fm++){ PIN(hs[fm]); PIN(hb[fm]); PIN(hx[fm]); }
  PIN(aR); PIN(aH);
  f32x4 acc[2][5], csa[3];
  #pragma unroll
  for(int k=0;k<5;k++) acc[0][k]=acc[1][k]=(f32x4){0.f,0.f,0.f,0.f};
  csa[0]=csa[1]=csa[2]=(f32x4){0.f,0.f,0.f,0.f};
  bf16x8 fa[2][2], fb[2][5], ga[2][4], gb[2][3]; s16x4 tl[3];
  auto rdA=[&](int m,int i,int s){
    const int xw=xab(i)>=65536, xo=xab(i)-65536*xw, wo=wab(i)-WS_(0);
    if(m==0) fa[s][0]=ldf(at(aA[xw][s],xo)); else if(m<=5) fb[s][m-1]=ldf(at(aB[s][m-1],wo)); else fa[s][1]=ldf(at(aA[xw][s],xo+2048)); };
  auto rdH=[&](int m,int n,int s){
    const int xw=xh(n)>=65536, xo=xh(n)-65536*xw, wo=wh(n)-WS_(0);
    if(m==0) ga[s][0]=ldf(at(hA[xw][s],xo));
    else if(m<=6){ const int c=(m-1)/2; if(m&1) tl[c]=ldtr1(at(hB[s][c],wo)); else gb[s][c]=cat(tl[c],ldtr1(at(hB[s][c],wo+768))); }
    else ga[s][m-6]=ldf(at(hA[xw][s],xo+(m-6)*2048)); };
  bar();
  TS(2); TSR(20);
  #pragma unroll
  for(int m=0;m<7;m++) rdA(m,0,0);
  GAP();
  f32x4 m0,m1,r0,r1; float c[5];
  sfor<10>([&](auto T){ constexpr int t=T.value;
    #pragma unroll
    for(int s=0;s<2;s++){
      const bool last=(s==1);
      if(last && t==9){
        #pragma unroll
        for(int f=0;f<3;f++) *(LAS float*)at(aC[f]+wc*320,0)=csa[f][0]; }
      wait_lgkm();
      if(last) bar();
      GAP();
      if(last && t==9){
        #pragma unroll
        for(int f=0;f<5;f++) c[f]=*(const LAS float*)at(aC[f],0)+*(const LAS float*)at(aC[f],320);
        m0=*(const LAS f32x4*)at(aR,0); m1=*(const LAS f32x4*)at(aR,16); r0=*(const LAS f32x4*)at(aR,512); r1=*(const LAS f32x4*)at(aR,528); }
      constexpr int NM= 13;
      #pragma unroll
      for(int m=0;m<NM;m++){
        if(!last){ if(m<3) csa[m]=mma(ones,fb[s][m],csa[m]); else { const int mm=m-3, fm=mm/5, f0=mm%5, fn=fm?4-f0:f0; acc[fm][fn]=mma(fa[s][fm],fb[s][fn],acc[fm][fn]); } }
        else if(m<10){ const int fm=m/5, f0=m%5, fn=fm?4-f0:f0; acc[fm][fn]=mma(fa[s][fm],fb[s][fn],acc[fm][fn]); }
        if(last && t==9){ if(m<10) rdH(m,0,0); }
        else if(m<7) rdA(m,last? t+1: t,s^1);
        GAP();
      }
    }
  });
  TS(3);
  { const float sg= j<8? 1.f: -1.f;
    r0*=sg; r1*=sg;
    #pragma unroll
    for(int f=0;f<5;f++){
      const f32x4 a=(acc[0][f]-m0*c[f])*r0, b=(acc[1][f]-m1*c[f])*r1;
      const u32x4 o={pk(a[0],a[1]),pk(a[2],a[3]),pk(b[0],b[1]),pk(b[2],b[3])};
      *(LAS u32x4*)at(aS[f],0)=o; }
  }
  GAP();
  TS(4); TSR(16);
  f32x4 hac[4][3], csh[3], c4[3];
  #pragma unroll
  for(int k=0;k<3;k++){ csh[k]=(f32x4){0.f,0.f,0.f,0.f};
    #pragma unroll
    for(int i=0;i<4;i++) hac[i][k]=(f32x4){0.f,0.f,0.f,0.f}; }
  float hm[4], hr[4];
  sfor<10>([&](auto T){ constexpr int t=T.value;
    #pragma unroll
    for(int s=0;s<2;s++){
      const bool last=(s==1);
      if(last && t==9){
        #pragma unroll
        for(int f=0;f<3;f++) *(LAS f32x4*)at(aH+wr*384,64*f)=csh[f]; }
      wait_lgkm();
      if(last) bar();
      GAP();
      if(last && t==9){
        #pragma unroll
        for(int f=0;f<3;f++) c4[f]=*(const LAS f32x4*)at(aH,64*f)+*(const LAS f32x4*)at(aH,384+64*f);
        #pragma unroll
        for(int fm=0;fm<4;fm++){ hm[fm]=*(const LAS float*)at(hs[fm],0); hr[fm]=*(const LAS float*)at(hs[fm],512); } }
      #pragma unroll
      for(int m=0;m<15;m++){
        if(!last){ if(m<3) csh[m]=mma(gb[s][m],ones,csh[m]); else { const int mm=m-3, fm=mm/3, f0=mm%3, fn=(fm&1)?2-f0:f0; hac[fm][fn]=mma(gb[s][fn],ga[s][fm],hac[fm][fn]); } }
        else if(m<12){ const int fm=m/3, f0=m%3, fn=(fm&1)?2-f0:f0; hac[fm][fn]=mma(gb[s][fn],ga[s][fm],hac[fm][fn]); }
        if(m<10 && !(last && t==9)) rdH(m,last? t+1: t,s^1);
        GAP();
      }
    }
  });
  TS(5);
  #pragma unroll
  for(int fm=0;fm<4;fm++){ const float zr=hr[fm]*-1.4426950408889634f;
    #pragma unroll
    for(int f=0;f<3;f++){ const f32x4 v=hac[fm][f]-c4[f]*hm[fm], a=v*hr[fm], z=v*zr; f32x4 e;
      #pragma unroll
      for(int k=0;k<4;k++) e[k]=__builtin_amdgcn_exp2f(z[k]);
      e+=1.f;
      #pragma unroll
      for(int k=0;k<4;k++) e[k]=__builtin_amdgcn_rcpf(e[k]);
      const f32x4 h=a*e; const u32x2 o={pk(h[0],h[1]),pk(h[2],h[3])};
      *(LAS u32x2*)at(hb[fm]+(hx[fm]^((3*wc+f)*32)),0)=o; } }
  wait_lgkm();
  bar();
  TS(6); TSR(17);
  return tag;
}

template<class Hook>
DI void y_loop(LAS char* lds, unsigned w, unsigned lane, f32x4 (&acc)[4][5], Hook hook){
  const unsigned wr=w>>1, wc=w&1, g=lane>>4, c16=lane&15;
  unsigned adA[2], adB[2];
  #pragma unroll
  for(int s=0;s<2;s++){ const unsigned ch=((4*s+g)^((lane>>1)&7))*16; adA[s]=(wr*64+c16)*128+ch; adB[s]=(wc*80+c16)*128+ch; }
  #pragma unroll
  for(int i=0;i<4;i++)
    #pragma unroll
    for(int k=0;k<5;k++) acc[i][k]=(f32x4){0.f,0.f,0.f,0.f};
  bf16x8 fa[2][4], fb[2][5];
  auto rd=[&](int m,int t,int ks,int bf){
    const LAS char* bA=lds+slotA(t); const LAS char* bB=lds+slotB(t);
    if(m==0) fa[bf][0]=ldf(bA+adA[ks]);
    else if(m<=5) fb[bf][m-1]=ldf(bB+adB[ks]+(m-1)*2048);
    else fa[bf][m-5]=ldf(bA+adA[ks]+(m-5)*2048); };
  #pragma unroll
  for(int m=0;m<9;m++) rd(m,0,0,0);
  GAP();
  sfor<NY>([&](auto T){ constexpr int t=T.value;
    sfor<2>([&](auto Sx){ constexpr int s=Sx.value; constexpr bool last=(s==1);
      wait_lgkm();
      if(last){ hook(T,ic<-1>{}); bar(); }
      GAP();
      sfor<20>([&](auto M){ constexpr int m=M.value, fm=m/5, f0=m%5, fn=(fm&1)?4-f0:f0;
        acc[fm][fn]=mma(fb[s][fn],fa[s][fm],acc[fm][fn]);
        if(m<9 && !(last && t==NY-1)) rd(m,last? t+1: t,last? 0: 1,s^1);
        hook(T,ic<s*20+m>{});
        GAP();
      });
    });
  });
}
struct GFrag { bf16x8 a[2][2], b[2][3]; };
struct GAddr { unsigned a[2][2], b[3][2]; };
DI GAddr gate_addr(unsigned w, unsigned lane){
  const unsigned wr=w>>1, wc=w&1, g=lane>>4, c16=lane&15; GAddr r;
  #pragma unroll
  for(int ks=0;ks<2;ks++)
    #pragma unroll
    for(int a=0;a<2;a++) r.a[ks][a]=(32*wr+16*(a^wc)+c16)*128+((4*ks+g)^((lane>>1)&7))*16;
  #pragma unroll
  for(int f=0;f<3;f++){ const unsigned b0=160*(8*g+(c16>>2))+8*(lane&3)+32*(wc? 4-f: f); r.b[f][0]=b0+(g&1)*640; r.b[f][1]=b0+640-(g&1)*640; }
  return r; }
DI void gate_rd(int m, const LAS char* lds, const GAddr& ad, int t, int h, GFrag& fr, s16x4 (&tl)[3]){
  const int ks=m/8, i=m%8; const LAS char* bA=lds+slotA(t)+h*8192; const LAS char* bB=lds+slotB(t)+(h*64+ks*32)*160;
  if(i==0) fr.a[ks][0]=ldf(bA+ad.a[ks][0]);
  else if(i<=6){ const int f=(i-1)/2; if(i&1) tl[f]=ldtr1(bB+ad.b[f][0]); else fr.b[ks][f]=cat(tl[f],ldtr1(bB+ad.b[f][1])); }
  else fr.a[ks][1]=ldf(bA+ad.a[ks][1]); }
template<class Hook>
DI void gate_loop(LAS char* lds, const GAddr& ad, GFrag (&fr)[2], f32x4 (&acc)[5], Hook hook){
  s16x4 tl[3];
  #pragma unroll
  for(int k=0;k<5;k++) acc[k]=(f32x4){0.f,0.f,0.f,0.f};
  sfor<NO>([&](auto T){ constexpr int t=NY+T.value;
    sfor<2>([&](auto Sx){ constexpr int s=Sx.value; constexpr bool last=(s==1);
      wait_lgkm();
      if(last){ hook(T,ic<-1>{}); bar(); }
      GAP();
      sfor<10>([&](auto M){ constexpr int m=M.value, ks=m/5, i=m%5, a=(i==2||i==3)? 1: 0, f= i==4? 2: (i&1);
        acc[i]=mma(fr[s].b[ks][f],fr[s].a[ks][a],acc[i]);
        if constexpr(!(last && t==NT-1)){
          if constexpr(m<6){ gate_rd(2*m,lds,ad,last? t+1: t,last? 0: 1,fr[s^1],tl); gate_rd(2*m+1,lds,ad,last? t+1: t,last? 0: 1,fr[s^1],tl); }
          else gate_rd(m+6,lds,ad,last? t+1: t,last? 0: 1,fr[s^1],tl); }
        hook(T,ic<s*10+m>{});
        GAP();
      });
    });
  });
}
struct Qd { u32x4 a,b; u32x2 c; };
DI u32x2 pk4(const f32x4& v){ return (u32x2){pk(v[0],v[1]),pk(v[2],v[3])}; }
template<int W> DI Qd pack_part(const f32x4 (&y)[4][5]){
  constexpr int wr=W>>1, wc=W&1, r0=2*wr+wc, r1=2*wr+1-wc, c0=wc? 4: 0, c1=wc? 3: 1;
  const u32x2 p0=pk4(y[r0][c0]), p1=pk4(y[r0][c1]), p2=pk4(y[r1][c0]), p3=pk4(y[r1][c1]);
  Qd r; r.a=(u32x4){p0[0],p0[1],p1[0],p1[1]}; r.b=(u32x4){p2[0],p2[1],p3[0],p3[1]}; r.c=pk4(y[r0][2]); return r; }
DI void add_part(float (&t)[20], const Qd& r){
  #pragma unroll
  for(int k=0;k<4;k++){ t[2*k]+=bflo(r.a[k]); t[2*k+1]+=bfhi(r.a[k]); t[8+2*k]+=bflo(r.b[k]); t[9+2*k]+=bfhi(r.b[k]); }
  #pragma unroll
  for(int k=0;k<2;k++){ t[16+2*k]+=bflo(r.c[k]); t[17+2*k]+=bfhi(r.c[k]); }
}
#define YSM 1
#define YS0 1
#define YSN 4
#define T_XV 10
#define T_YF 4
#define T_PY 7
#define T_CY 9

DI void p2_mfma(const Prm& p, LAS char* lds, int q, int pm, int pn, unsigned w, unsigned lane, u64 tag TSP){
  const int tile=pm*4+pn;
  const srd_t sY=mksrd(p.ws+WS_YX);
  const unsigned wr=w>>1, wc=w&1, g=lane>>4, c16=lane&15;
  const unsigned sbase=((tile*4+w)*4+q)*4*2560, rbase=(tile*4+q)*16*2560+w*2560;
  const unsigned pY=WS_YF+((tile*4+q)*4+(lane&3))*8;
  u64 pv=0;
  Qd yq[4];
  { f32x4 acc[4][5];
    const unsigned pH= lane<32? WS_HF+(pm*16+(lane&15))*8: WS_ABF+((2*pn+(q&1))*32+(lane&31))*8;
    y_loop(lds,w,lane,acc,[&](auto T,auto M){ constexpr int t=T.value, m=M.value;
      if constexpr(t==T_PH && m==17){ if(w==1) poll_issue(pv,p.ws,pH); }
      if constexpr(t==NY-4 && m==-1){ if(w==1){ wait_vm<0>(); poll_check(pv,p.ws,pH,tag); } } });
    TS(7); TSR(18);
    yq[0]=pack_part<0>(acc); yq[1]=pack_part<1>(acc); yq[2]=pack_part<2>(acc); yq[3]=pack_part<3>(acc); }
  const GAddr ad=gate_addr(w,lane); GFrag fr[2];
  { s16x4 tl[3];
    #pragma unroll
    for(int m=0;m<16;m++) gate_rd(m,lds,ad,NY,0,fr[0],tl); }
  GAP();
  auto ysend=[&](auto K,auto P){ constexpr int k=K.value, pt=P.value;
    if((int)w==q){ LAS char* d=lds+YOWN+k*2560;
      if constexpr(pt==0) *(LAS u32x4*)(d+lane*16)=yq[k].a; else if constexpr(pt==1) *(LAS u32x4*)(d+1024+lane*16)=yq[k].b; else *(LAS u32x2*)(d+2048+lane*8)=yq[k].c;
    } else { const unsigned b=sbase+k*2560;
      if constexpr(pt==0) __builtin_amdgcn_raw_buffer_store_b128(yq[k].a,sY,b+lane*16,0,16);
      else if constexpr(pt==1) __builtin_amdgcn_raw_buffer_store_b128(yq[k].b,sY,b+1024+lane*16,0,16);
      else __builtin_amdgcn_raw_buffer_store_b64(yq[k].c,sY,b+2048+lane*8,0,16); } };
  if constexpr(YSM==0) sfor<12>([&](auto N){ ysend(ic<N.value/3>{},ic<N.value%3>{}); });
  else {
    #pragma unroll
    for(int k=0;k<4;k++) asm volatile("":"+v"(yq[k].a),"+v"(yq[k].b),"+v"(yq[k].c)); }
  GAP();
  TS(8);
  f32x4 og[5]; Qd y[3]; u32x2 xv[5];
  const unsigned row0=pm*128+(q>>1)*64+32*wr+c16+16*wc, row1=row0+16-32*wc, col0=pn*160+(q&1)*80+4*g;
  const unsigned oo[5]={row0*D_+col0+(wc? 64: 0), row0*D_+col0+(wc? 48: 16), row1*D_+col0+(wc? 64: 0), row1*D_+col0+(wc? 48: 16), row0*D_+col0+32};
  auto fetch=[&](auto N){ constexpr int n=N.value;
    if constexpr(n<9){ constexpr int k=n/3, pt=n%3; const unsigned b=rbase+((q+1+k)&3)*4*2560;
      if constexpr(pt==0) y[k].a=__builtin_amdgcn_raw_buffer_load_b128(sY,b+lane*16,0,16);
      else if constexpr(pt==1) y[k].b=__builtin_amdgcn_raw_buffer_load_b128(sY,b+1024+lane*16,0,16);
      else y[k].c=__builtin_amdgcn_raw_buffer_load_b64(sY,b+2048+lane*8,0,16); }
    else if constexpr(n<14) xv[n-9]=*(const u32x2*)(p.x+oo[n-9]); };
  gate_loop(lds,ad,fr,og,[&](auto T,auto M){ constexpr int t=T.value, m=M.value;
    if constexpr(YSM==1 && m>=0){ constexpr int n=(2*t+m/10-YS0)*YSN+(m%10)/2;
      if constexpr(2*t+m/10>=YS0 && (m&1)==1 && (m%10)/2<YSN && n<12) ysend(ic<n/3>{},ic<n%3>{}); }
    if constexpr(t==T_YF && m==-1){ wait_vm<0>(); if(lane==0) st_flag(p.ws,WS_YF+((tile*4+w)*4+q)*8,tag); }
    if constexpr(t==T_PY && m==8) poll_issue(pv,p.ws,pY);
    if constexpr(t==T_XV && m>=11 && m<16) fetch(ic<9+m-11>{});
    if constexpr(t==T_CY && m==-1){ wait_vm<0>(); poll_check(pv,p.ws,pY,tag); }
    if constexpr(m>=0){ constexpr int hh=2*(t-T_CY)+m/10-1;
      if constexpr(hh>=0 && hh<3 && (m%10)%3==1 && (m%10)<9) fetch(ic<3*hh+(m%10)/3>{}); } });
  TS(9); TSR(19);
  { const LAS char* d=lds+YOWN+w*2560; Qd y0; y0.a=*(const LAS u32x4*)(d+lane*16); y0.b=*(const LAS u32x4*)(d+1024+lane*16); y0.c=*(const LAS u32x2*)(d+2048+lane*8);
    float ty[20];
    #pragma unroll
    for(int k=0;k<20;k++) ty[k]=0.f;
    add_part(ty,y0);
    #pragma unroll
    for(int k=0;k<3;k++) add_part(ty,y[k]);
    TS(10);
    #pragma unroll
    for(int k=0;k<5;k++){ u32x2 o;
      #pragma unroll
      for(int e=0;e<2;e++){ const float o0=og[k][2*e], o1=og[k][2*e+1];
        o[e]=pk(bflo(xv[k][e])+ty[4*k+2*e]*(o0*sigm(o0)), bfhi(xv[k][e])+ty[4*k+2*e+1]*(o1*sigm(o1))); }
      *(u32x2*)(p.out+oo[k])=o; }
  }
  TS(11);
}
__global__ __launch_bounds__(512,2) void fnet_mlp_fused(Prm p){
  extern __shared__ __attribute__((aligned(1024))) char smem[];
  LAS char* lds=(LAS char*)smem;
  const unsigned tid=threadIdx.x, lane=tid&63, w=rfl(tid>>6);
  const int b=blockIdx.x, x8=b&7, r=b>>3;
  const int q=x8>>1, pm=(x8&1)*8+(r>>2), pn=r&3, j=q*4+pn;
  TSR(14); TS(0);
  if(w>=4){
    const unsigned lw=w-4;
    const u64 tag= lw<2? loader<3>(p,lds,pm,j,q,pn,lw,lane TSA) : loader<2>(p,lds,pm,j,q,pn,lw,lane TSA);
    TS(8);
    if(b==0 && lw==0){
      #pragma unroll 1
      for(int i=0;i<4;i++) wait_flags(p.ws,WS_ABF+(i*64+lane)*8,tag);
      if(lane==0) st_flag(p.ws,0,tag);
    }
  } else {
    const u64 tag=p1_mfma(p,lds,pm,j,w,lane TSA);
    p2_mfma(p,lds,q,pm,pn,w,lane,tag TSA);
  }
  TSR(15); TSPUT();
}

extern "C" void kernel_launch(void* const* d_in, const int* in_sizes, int n_in, void* d_out, int out_size, void* d_ws, size_t ws_size, hipStream_t stream) {
  Prm p; p.x=(const u16*)d_in[0]; p.wsre=(const u16*)d_in[1]; p.wsim=(const u16*)d_in[2]; p.wdre=(const u16*)d_in[3]; p.wdim=(const u16*)d_in[4];
  p.w1=(const u16*)d_in[6]; p.w2=(const u16*)d_in[7]; p.out=(u16*)d_out; p.ws=(char*)d_ws+WSOFF;
  (void)hipFuncSetAttribute((const void*)fnet_mlp_fused, hipFuncAttributeMaxDynamicSharedMemorySize, LDS_BYTES);
  fnet_mlp_fused<<<256,512,LDS_BYTES,stream>>>(p);
}
```

```cpp
#define CPF 11
#include <hip/hip_runtime.h>
#include <stdint.h>
#include <type_traits>
#include <utility>
typedef unsigned short u16;
typedef unsigned long long u64;
typedef float f32x4 __attribute__((ext_vector_type(4)));
typedef float f32x2 __attribute__((ext_vector_type(2)));
typedef __bf16 bf16x8 __attribute__((ext_vector_type(8)));
typedef __bf16 bf16x2 __attribute__((ext_vector_type(2)));
typedef short s16x4 __attribute__((ext_vector_type(4)));
typedef short s16x8 __attribute__((ext_vector_type(8)));
typedef unsigned u32x4 __attribute__((ext_vector_type(4)));
typedef unsigned u32x2 __attribute__((ext_vector_type(2)));
#define DI __device__ __forceinline__
#define GAP() __builtin_amdgcn_sched_barrier(0)
#define LAS __attribute__((address_space(3)))
#define GAS __attribute__((address_space(1)))
typedef GAS u64 gu64;
typedef GAS unsigned gu32;
typedef __amdgpu_buffer_rsrc_t srd_t;

constexpr int S_=2048, D_=640, F_=1536;
constexpr unsigned WS_ABF=8192, WS_HF=12288, WS_YF=16384, WS_RST=65536;
constexpr unsigned WS_ABT=1u<<20, WS_H=8u<<20, WS_YX=16u<<20, WS_W2=30u<<20;
constexpr int XS(int i){ return i*16384; }
constexpr int WS_(int i){ return 98304+i*12288; }
constexpr int ABS1=XS(4), ABS2=155648, AUX=159744, STG=AUX, ROWM=AUX+2048, ROWR=ROWM+512, CSX=ROWM+1024, LDS_BYTES=163840;
constexpr int xab(int i){ return XS(i%6); }
constexpr int wab(int i){ return WS_(i%5); }
constexpr int xh(int n){ return n<5? XS((5+n)%6): n==5? XS(5): XS(n-6); }
constexpr int wh(int n){ constexpr int m[10]={0,1,2,3,0,4,1,2,3,4}; return WS_(m[n]); }
constexpr int PA[5]={XS(4),XS(0),XS(1),XS(2),XS(3)}, PB[4]={81920,102400,122880,143360};
constexpr int HST0=PA[4], HST1=PB[3], YOWN=PA[0];
constexpr unsigned SPIN_MAX=40000;
#define WSOFF 0
#define ROTEXPR ((pn+pm)&3)
#define YG 4
#define OROT rot
#define YOFF ((pm+q)&3)
#define OOFF ((pm+q)&3)
#define K0EXPR ((pm+3*j)%10)
constexpr int NY=16, NO=12, NT=NY+NO;
constexpr int slotA(int t){ return t<NY? PA[t%5]: PA[1+(t-NY)%4]; }
constexpr int slotB(int t){ return PB[t&3]; }

#define TS(i)
#define TB(t)
#define TSR(l)
#define TSPUT()
#define TW(t)
#define TX(t)
#define TSP
#define TSA

template<int I> using ic=std::integral_constant<int,I>;
template<int... I,class F> DI void sfor_(std::integer_sequence<int,I...>, F&& f){ (f(ic<I>{}),...); }
template<int N,class F> DI void sfor(F&& f){ sfor_(std::make_integer_sequence<int,N>{},f); }
DI unsigned rfl(unsigned x){ return __builtin_amdgcn_readfirstlane(x); }
DI float bflo(unsigned v){ return __uint_as_float(v<<16); }
DI float bfhi(unsigned v){ return __uint_as_float(v&0xffff0000u); }
DI unsigned pk(float lo,float hi){ f32x2 v={lo,hi}; bf16x2 b=__builtin_convertvector(v,bf16x2); return __builtin_bit_cast(unsigned,b); }
DI float sigm(float v){ return __builtin_amdgcn_rcpf(1.f+__builtin_amdgcn_exp2f(-1.4426950408889634f*v)); }
template<int N> DI void wait_vm(){ asm volatile("s_waitcnt vmcnt(%0)"::"n"(N):"memory"); }
DI void bar(){ __builtin_amdgcn_s_barrier(); }
DI void wait_lgkm(){ __builtin_amdgcn_s_waitcnt(0xC07F); }
constexpr unsigned BIAS=4096;
#define DMA1(O,X) "buffer_load_dwordx4 %" #O ", %1, %2 offen" X " lds"
#define DMA_GROUP(X) \
  if constexpr(N==1) asm volatile("s_mov_b32 m0, %0\n\ts_nop 0\n\tbuffer_load_dwordx4 %3, %1, %2 offen" X " lds" :: "s"(m0v),"s"(srd),"s"(soff),"v"(v[0]) : "memory"); \
  if constexpr(N==2) asm volatile("s_mov_b32 m0, %0\n\ts_nop 0\n\tbuffer_load_dwordx4 %3, %1, %2 offen" X " lds\n\tbuffer_load_dwordx4 %4, %1, %2 offen offset:1024" X " lds" :: "s"(m0v),"s"(srd),"s"(soff),"v"(v[0]),"v"(v[1]) : "memory"); \
  if constexpr(N==3) asm volatile("s_mov_b32 m0, %0\n\ts_nop 0\n\tbuffer_load_dwordx4 %3, %1, %2 offen" X " lds\n\tbuffer_load_dwordx4 %4, %1, %2 offen offset:1024" X " lds\n\tbuffer_load_dwordx4 %5, %1, %2 offen offset:2048" X " lds" :: "s"(m0v),"s"(srd),"s"(soff),"v"(v[0]),"v"(v[1]),"v"(v[2]) : "memory"); \
  if constexpr(N==4) asm volatile("s_mov_b32 m0, %0\n\ts_nop 0\n\tbuffer_load_dwordx4 %3, %1, %2 offen" X " lds\n\tbuffer_load_dwordx4 %4, %1, %2 offen offset:1024" X " lds\n\tbuffer_load_dwordx4 %5, %1, %2 offen offset:2048" X " lds\n\tbuffer_load_dwordx4 %6, %1, %2 offen offset:3072" X " lds" :: "s"(m0v),"s"(srd),"s"(soff),"v"(v[0]),"v"(v[1]),"v"(v[2]),"v"(v[3]) : "memory");
#define SC1S " sc1"
template<int N,int POL> DI void dma_group(const __amdgpu_buffer_rsrc_t& srd, const unsigned* v, unsigned soff, unsigned m0v){
  if constexpr(POL==1){ DMA_GROUP(SC1S) } else if constexpr(POL==2){ DMA_GROUP(" nt") } else { DMA_GROUP("") }
}
template<int POL> DI void dma5(const __amdgpu_buffer_rsrc_t& srd, const unsigned* v, unsigned soff, unsigned m0v){
  dma_group<4,POL>(srd,v,soff,m0v); dma_group<1,POL>(srd,v+4,soff,m0v+4096u);
}
#define NTX 0
#define NTD 0
#define NT1 0
#define NTS 0
#define NTSF NTS
#define NT2 0
DI srd_t mksrd(const void* p){ return __builtin_amdgcn_make_buffer_rsrc((void*)p,0,0x7ffffff0,0x00020000); }
DI srd_t mksrd_lo(const void* p){ return mksrd((const char*)p-BIAS); }
DI void st_flag(char* ws, unsigned off, u64 tag){ __hip_atomic_store((gu64*)(ws+off), tag, __ATOMIC_RELAXED, __HIP_MEMORY_SCOPE_AGENT); }
DI void st_f32(char* ws, unsigned off, float v){ __hip_atomic_store((gu32*)(ws+off), __float_as_uint(v), __ATOMIC_RELAXED, __HIP_MEMORY_SCOPE_AGENT); }
DI u64 ld_flag(char* ws, unsigned off){ return __hip_atomic_load((gu64*)(ws+off), __ATOMIC_RELAXED, __HIP_MEMORY_SCOPE_AGENT); }
DI void wait_flags(char* ws, unsigned off, u64 tag){
  #pragma unroll 1
  for(unsigned s=0;s<SPIN_MAX;s++){ const u64 v=ld_flag(ws,off); if(__all(v==tag)) break; __builtin_amdgcn_s_sleep(2); }
}
DI u64 get_tag(const char* ws){ u64 ep; asm volatile("s_load_dwordx2 %0, %1, 0x0\n\ts_waitcnt lgkmcnt(0)":"=s"(ep):"s"(ws):"memory"); return ep+1; }
DI void poll_issue(u64& v, char* ws, unsigned off){ const char* a=ws+off; asm volatile("global_load_dwordx2 %0, %1, off sc1":"=v"(v):"v"(a):"memory"); }
DI void poll_check(u64& v, char* ws, unsigned off, u64 tag){ asm volatile("":"+v"(v)); if(!__all(v==tag)) wait_flags(ws,off,tag); }
DI const LAS char* at(unsigned reg,int off){ return (const LAS char*)(size_t)reg+off; }
#define PIN(x) asm volatile("":"+v"(x))
DI bf16x8 ldf(const LAS char* a){ return *(const LAS bf16x8*)a; }
DI bf16x8 ldtr(const LAS char* a, int step){
  const s16x4 lo=__builtin_amdgcn_ds_read_tr16_b64_v4i16((LAS s16x4*)a), hi=__builtin_amdgcn_ds_read_tr16_b64_v4i16((LAS s16x4*)(a+step));
  return __builtin_bit_cast(bf16x8,(s16x8)__builtin_shufflevector(lo,hi,0,1,2,3,4,5,6,7));
}
DI s16x4 ldtr1(const LAS char* a){ return __builtin_amdgcn_ds_read_tr16_b64_v4i16((LAS s16x4*)a); }
DI bf16x8 cat(s16x4 lo, s16x4 hi){ return __builtin_bit_cast(bf16x8,(s16x8)__builtin_shufflevector(lo,hi,0,1,2,3,4,5,6,7)); }
DI f32x4 mma(bf16x8 a, bf16x8 b, f32x4 c){ return __builtin_amdgcn_mfma_f32_16x16x32_bf16(a,b,c,0,0,0); }

template<int CTRL> DI float dpp_add(float v){ return v+__int_as_float(__builtin_amdgcn_update_dpp(0,__float_as_int(v),CTRL,0xf,0xf,true)); }
DI float sum16(float v){ v=dpp_add<0xB1>(v); v=dpp_add<0x4E>(v); v=dpp_add<0x141>(v); return dpp_add<0x140>(v); }

struct Prm { const u16* x; const u16* wsre; const u16* wsim; const u16* wdre; const u16* wdim; const u16* w1; const u16* w2; u16* out; char* ws; };

enum { K_X, K_WD, K_XR, K_W1, K_YA, K_YB, K_OA, K_OB, K_STAT, K_STCHK, K_ABS, K_FAB, K_PAB, K_CAB, K_HRD, K_HS, K_FH, K_PF, K_W2L, K_W2S };
struct Op { int k, i, step; };
struct Sched { Op op[200]; int n; };
constexpr int TB1=20, TP2=21, TEND=TP2+NT;
#define ST_FAB 15
#define ST_PAB 18
#define ST_YA0 16
#define ST_YA1 17
#define ST_YA2 19
#define ST_YA3 19
#define PFU 4
#define PFY 0
#define ST_PFY 9
#define ST_PFW 100
#define ST_PF1 100
#define ST_W2L 6
#define ST_W2S 9
#define ST_FH 8
#define T_PH 10
#define YPAIR 0
constexpr Sched make_sched(){
  Sched s{}; auto add=[&](int k,int i,int st){ s.op[s.n].k=k; s.op[s.n].i=i; s.op[s.n].step=st; s.n++; };
#define PRO 1
  if(PRO==0){ add(K_X,0,-2); add(K_WD,0,-2); add(K_X,1,-2); add(K_WD,1,-2); add(K_X,2,-1); add(K_WD,2,-1); add(K_X,3,-1); }
  if(PRO==1){ add(K_X,0,-2); add(K_WD,0,-2); add(K_X,1,-2); add(K_X,2,-2); add(K_WD,1,-2); add(K_WD,2,-1); add(K_X,3,-1); }
  if(PRO==2){ add(K_X,0,-2); add(K_WD,0,-2); add(K_X,1,-2); add(K_WD,1,-2); add(K_WD,2,-2); add(K_X,2,-1); add(K_X,3,-1); }
  for(int st=0;st<20;st++){
    if(st==4) add(K_STAT,0,st);
    if(st==ST_FAB) add(K_FAB,0,st);
    if(st==ST_PAB) add(K_PAB,0,st);
    if(st<=6) add(K_WD,st+3,st);
    if(st<=5) add(K_X,st+4,st);
    if(st>=5 && st<=8) add(K_W1,st-5,st);
    if(st==ST_W2L) add(K_W2L,0,st);
    if(st==8) add(K_STCHK,0,st);
    if(st==ST_W2S) add(K_W2S,0,st);
    if(st==ST_PFY && PFY>0) add(K_PF,0,st);
    if(st==ST_PFY && PFY>1) add(K_PF,1,st);
    if(st==ST_PFW) add(K_PF,2,st);
    if(st==ST_PF1) add(K_PF,3,st);
    if(st==10){ add(K_W1,4,st); add(K_ABS,0,st); }
    if(st==11){ add(K_XR,0,st); add(K_W1,5,st); }
    if(st>=12 && st<=15){ add(K_XR,st-11,st); add(K_W1,st-6,st); }
    if(st==ST_YA0) add(K_YA,0,st);
    if(st==ST_YA1) add(K_YA,1,st);
    if(st==19){ add(K_CAB,0,st); add(K_YB,0,st); add(K_YB,1,st);
      if(ST_YA2==19) add(K_YA,2,st); add(K_YB,2,st); if(ST_YA3==19) add(K_YA,3,st); }
    if(st==ST_YA2 && st!=19) add(K_YA,2,st);
    if(st==ST_YA3 && st!=19) add(K_YA,3,st);
  }
  add(K_HRD,0,TB1);
  for(int t=0;t<NT;t++){ const int st=TP2+t;
    if(t==ST_FH) add(K_FH,0,st);
    if(t==0){ add(K_YB,3,st); if(YPAIR) add(K_YA,5-1,st); else add(K_YA,4,st); }
    if(t<6 && !(YPAIR && t==0)) add(K_HS,t,st);
    if(YPAIR==0){
      if(t+4<NY) add(K_YB,t+4,st);
      if(t+5<NY) add(K_YA,t+5,st); }
    if(YPAIR==1){
      if(t+4<NY) add(K_YB,t+4,st);
      if(t==0) add(K_YA,5,st);
      if(t>0 && (t&1)==0){ if(t+4<NY) add(K_YA,t+4,st); if(t+5<NY) add(K_YA,t+5,st); } }
    if(YPAIR==2){
      if(t==0) add(K_YA,5,st);
      if(t>0 && (t&1)==0){ if(t+4<NY) add(K_YA,t+4,st); if(t+5<NY) add(K_YA,t+5,st); }
      if(t+4<NY) add(K_YB,t+4,st); }
    if(YPAIR && t==0) add(K_HS,0,st);
    if(t+4>=NY && t+4<NT){ add(K_OA,t+4,st); add(K_OB,t+4,st); }
  }
  return s;
}
constexpr Sched SCH=make_sched();
constexpr int op_cnt(const Op& o,int nwd){
  return o.k==K_WD? nwd: o.k==K_W1? 3: (o.k==K_X||o.k==K_XR||o.k==K_YA||o.k==K_OA)? 4: (o.k==K_YB||o.k==K_OB||o.k==K_ABS)? 5: (o.k==K_W2L||o.k==K_W2S)? 2: (o.k==K_HS||o.k==K_PAB||(o.k==K_PF && o.i<2))? 1: 0; }
constexpr int first_use(const Op& o){
  return (o.k==K_X||o.k==K_WD)? o.i-1: o.k==K_W1? o.i+9: o.k==K_XR? o.i+14: (o.k==K_YA||o.k==K_YB||o.k==K_OA||o.k==K_OB)? TB1+o.i: o.k==K_STAT? 8: 1000; }
constexpr int last_use(const Op& o){
  return o.k==K_X? (o.i>=5? o.i+5: o.i): o.k==K_WD? o.i: o.k==K_W1? o.i+10: o.k==K_XR? o.i+15: (o.k==K_YA||o.k==K_YB||o.k==K_OA||o.k==K_OB)? TP2+o.i: o.k==K_STAT? 9: -1000; }
constexpr int lds_lo(const Op& o){
  return o.k==K_X? xab(o.i): o.k==K_WD? wab(o.i): o.k==K_W1? wh(o.i): o.k==K_XR? xh(o.i+5): (o.k==K_YA||o.k==K_OA)? slotA(o.i): (o.k==K_YB||o.k==K_OB)? slotB(o.i): o.k==K_STAT? STG: -1; }
constexpr int lds_sz(const Op& o){
  return o.k==K_WD? 10240: o.k==K_W1? 12288: (o.k==K_X||o.k==K_XR||o.k==K_YA||o.k==K_OA)? 16384: (o.k==K_YB||o.k==K_OB)? 20480: o.k==K_STAT? 2048: 0; }
constexpr bool needed(const Op& o,int tau){ return first_use(o)==tau; }
constexpr int last_needed(int tau){ int l=-1; for(int n=0;n<SCH.n;n++) if(SCH.op[n].step<tau && needed(SCH.op[n],tau)) l=n; return l; }
constexpr int allowed(int tau,int nwd){ const int l=last_needed(tau); if(l<0) return -1; int c=0; for(int n=l+1;n<SCH.n;n++) if(SCH.op[n].step<tau) c+=op_cnt(SCH.op[n],nwd); return c; }
constexpr int find_op(int k,int i){ for(int n=0;n<SCH.n;n++) if(SCH.op[n].k==k && SCH.op[n].i==i) return n; return 1<<20; }
constexpr bool done_by(int k,int i,int tau){ const int p=find_op(k,i); for(int t=-1;t<=tau;t++) if(last_needed(t)>=p) return true; return false; }
constexpr int behind(int k0,int k1,int nwd){ int c=0; for(int n=find_op(k0,0)+1;n<find_op(k1,0);n++) c+=op_cnt(SCH.op[n],nwd); return c; }
constexpr int step_begin(int st){ int n=0; while(n<SCH.n && SCH.op[n].step<st) n++; return n; }
struct Reg { int lo, sz, from, to; };
constexpr Reg STAGED[7]={ {ABS1,16384,9,11}, {ABS2,4096,9,11}, {HST0,16384,19,TP2}, {HST1,16384,19,TP2}, {YOWN,10240,TP2+NY-1,1000},
                          {ROWM,1024,8,TB1}, {CSX,1024,7,TB1} };
constexpr bool sched_ok(){
  for(int n=0;n<SCH.n;n++){ const Op& a=SCH.op[n];
    if(n && a.step<SCH.op[n-1].step) return false;
    if(lds_sz(a)==0) continue;
    if(a.step>=first_use(a)) return false;
    for(int m=0;m<SCH.n;m++){ const Op& b=SCH.op[m]; if(m==n || lds_sz(b)==0) continue;
      if(lds_lo(a)<lds_lo(b)+lds_sz(b) && lds_lo(b)<lds_lo(a)+lds_sz(a) && a.step<last_use(b) && b.step<last_use(a)) return false; }
    for(const Reg& r: STAGED) if(lds_lo(a)<r.lo+r.sz && r.lo<lds_lo(a)+lds_sz(a) && a.step<r.to && r.from<last_use(a)) return false; }
  for(int k: {K_X,K_WD,K_W1}) for(int i=0;i<10;i++) if(find_op(k,i)>=SCH.n) return false;
  for(int i=0;i<5;i++) if(find_op(K_XR,i)>=SCH.n) return false;
  for(int i=0;i<NT;i++) if(find_op(i<NY? K_YA: K_OA,i)>=SCH.n || find_op(i<NY? K_YB: K_OB,i)>=SCH.n) return false;
  return true; }
static_assert(sched_ok(),"LDS regions are reused only behind the barrier that ends their last read");
static_assert(find_op(K_W2L,0)<find_op(K_W2S,0) && find_op(K_W2S,0)<find_op(K_ABS,0),"the a/b flag also covers the re-laid part of w2");
static_assert(done_by(K_ABS,0,ST_FAB) && done_by(K_HS,5,TP2+ST_FH),"a flag follows its completed stores");
static_assert(find_op(K_CAB,0)<find_op(K_YB,0) && find_op(K_PAB,0)<find_op(K_CAB,0),"a/b tiles are loaded behind the check of their flags");
static_assert(T_PH<NY-4 && SCH.op[find_op(K_OA,NY)].step>=TP2+NY-4,"h tiles are loaded behind the check of their flags");

template<int NWD>
DI u64 loader(const Prm& p, LAS char* lds, int pm, int j, int q, int pn, unsigned lw, unsigned lane TSP){
  const unsigned ldsb=(unsigned)(size_t)lds;
  const unsigned r=lane>>3;
  const int rot=ROTEXPR, k0=K0EXPR, orot=OROT, yoff=YOFF, ooff=OOFF;
  u64 tag, pv=0;
  const srd_t sA=mksrd_lo(p.x+(size_t)pm*128*D_), sD=mksrd_lo((j<8? p.wdre: p.wdim)+(size_t)(j&7)*80*D_), sW=mksrd_lo(p.w1+j*96);
  const unsigned q0= NWD==3? 3*lw: 2*lw+2;
  unsigned vX[4], vD[NWD], vW[3];
  #pragma unroll
  for(int i=0;i<4;i++) vX[i]=(32*lw+16*(i&1)+4*(i>>1)+8*(r>>2)+(r&3))*(D_*2)+((lane&7)^((i&1)*4+(r>>1)))*16+BIAS-1024*i;
  #pragma unroll
  for(int i=0;i<NWD;i++) vD[i]=((q0+i)*8+r)*(D_*2)+((lane&7)^(((q0+i)&1)*4+(r>>1)))*16+BIAS-1024*i;
  #pragma unroll
  for(int i=0;i<3;i++){ const unsigned c=i*64+lane, row=c/12, ch=c%12; vW[i]=(16*lw+row)*(F_*2)+32*((ch>>1)^((row>>3)&1))+16*(ch&1)+BIAS-1024*i; }
  auto ktile=[&](int i){ const int kt=k0+i; return kt>=10? kt-10: kt; };
  auto ldx=[&](int i,int dst){ dma_group<4,NTX>(sA,vX,ktile(i)*128u,ldsb+lw*4096u+dst); };
  auto ldwd=[&](int i){ dma_group<NWD,NTD>(sD,vD,ktile(i)*128u,ldsb+q0*1024u+wab(i)); };
  constexpr int NPRE=step_begin(-1);
  TS(1);
  sfor<NPRE>([&](auto N){ constexpr Op o=SCH.op[N.value]; static_assert(o.k==K_X||o.k==K_WD,"prologue");
    if constexpr(o.k==K_X) ldx(o.i,xab(o.i)); else ldwd(o.i); });
  TS(2);
  tag=get_tag(p.ws);
  const srd_t sWs=mksrd_lo((q<2? p.wsre: p.wsim)+(size_t)pm*128*S_+(q&1)*1024), sT=mksrd_lo(p.ws+WS_ABT+(size_t)pn*160*8192+q*2048);
  const srd_t sH=mksrd_lo(p.ws+WS_H+(size_t)pm*48*8192+(q>>1)*4096), sW2=mksrd_lo(p.ws+WS_W2+(2*pn+(q&1))*245760), sWk=mksrd(p.ws);
  unsigned vA[4], vB[5], vH[4], vW2[5];
  #pragma unroll
  for(int i=0;i<4;i++){ const unsigned c=(lane&7)^((i&1)*4+(r>>1));
    vA[i]=(32*lw+8*i+r)*(S_*2)+c*16+BIAS-1024*i;
    vH[i]=(2*(lw>>1)+(c>>2))*8192+(32*(lw&1)+8*i+r)*64+(c&3)*16+BIAS-1024*i; }
  #pragma unroll
  for(int i=0;i<5;i++){ const unsigned bi=BIAS-(i<4? 1024*i: 0);
    vB[i]=((5*lw+i)*8+r)*8192+((lane&7)^(((lw+i)&1)*4+(r>>1)))*16+bi;
    vW2[i]=lw*5120+lane*16+BIAS+(i<4? 0: 4096); }
  u32x4 wv[2]; unsigned wsrc[2], wdst[2];
  #pragma unroll
  for(int i=0;i<2;i++){ const unsigned cc=i*60+(lane<60? lane: 59), rho=(2*pm+(j&1))*48+12*lw+cc/10, ch=cc%10, row=rho^(((rho>>3)&1)<<2), sl=j>>1;
    wsrc[i]=row*(D_*2)+((sl>>1)*160+(sl&1)*80)*2+ch*16; wdst[i]=WS_W2+sl*245760+rho*160+ch*16; }
  const srd_t sW2s=mksrd(p.w2);
  const unsigned lA2=ldsb+lw*4096u, lB2=ldsb+lw*5120u;
  u32x4 hv[6]; unsigned pfv=0;
  const unsigned pAB=WS_ABF+((((q>>1)*4+pn)*16+(q&1)*8)*2+(lane&15))*8;
  auto exec=[&](auto N){ constexpr Op o=SCH.op[N.value];
    if constexpr(o.k==K_X) ldx(o.i,xab(o.i));
    if constexpr(o.k==K_XR) ldx(o.i,xh(o.i+5));
    if constexpr(o.k==K_WD) ldwd(o.i);
    if constexpr(o.k==K_W1) dma_group<3,NT1>(sW,vW,ktile(o.i>=5? o.i-5: o.i+5)*(64u*F_*2),ldsb+lw*3072u+wh(o.i));
    if constexpr(o.k==K_YA){ const int i2=(o.i+yoff)&15, u=(i2&~(YG-1))|((i2+rot)&(YG-1));
      if constexpr(NTSF!=NTS){ if(o.i<=(YPAIR?1:0) || (i2&(YG-1))<=(YPAIR?1:0)) dma_group<4,NTSF>(sWs,vA,u*128u,lA2+slotA(o.i)); else dma_group<4,NTS>(sWs,vA,u*128u,lA2+slotA(o.i)); }
      else dma_group<4,NTS>(sWs,vA,u*128u,lA2+slotA(o.i)); }
    if constexpr(o.k==K_YB){ const int i2=(o.i+yoff)&15, u=(i2&~(YG-1))|((i2+rot)&(YG-1)); dma5<1>(sT,vB,u*128u,lB2+slotB(o.i)); }
    if constexpr(o.k==K_OA){ int i2=o.i-NY+ooff; i2= i2>=NO? i2-NO: i2; const int u=(i2&~3)|((i2+orot)&3); dma_group<4,1>(sH,vH,u*32768u,lA2+slotA(o.i)); }
    if constexpr(o.k==K_OB){ int i2=o.i-NY+ooff; i2= i2>=NO? i2-NO: i2; const int u=(i2&~3)|((i2+orot)&3); dma5<1>(sW2,vW2,u*20480u,lB2+slotB(o.i)); }
    if constexpr(o.k==K_STAT){ if(NWD==3 && lw==0){ const unsigned v0=WS_RST+pm*2048+lane*16, vs[2]={v0,v0}; dma_group<2,1>(sWk,vs,0,ldsb+STG); } }
    if constexpr(o.k==K_STCHK){ if(NWD==3 && lw==0){
        u32x4 a=*(const LAS u32x4*)(lds+STG+lane*16), b=*(const LAS u32x4*)(lds+STG+1024+lane*16);
        const unsigned tg=(unsigned)tag;
        if(!__all(a[1]==tg && a[3]==tg && b[1]==tg && b[3]==tg)){
          #pragma unroll 1
          for(unsigned sp=0;sp<SPIN_MAX;sp++){ asm volatile("":::"memory");
            a=__builtin_amdgcn_raw_buffer_load_b128(sWk,WS_RST+pm*2048+lane*16,0,16); b=__builtin_amdgcn_raw_buffer_load_b128(sWk,WS_RST+pm*2048+1024+lane*16,0,16);
            if(__all(a[1]==tg && a[3]==tg && b[1]==tg && b[3]==tg)) break; } }
        LAS unsigned* rm=(LAS unsigned*)(lds+ROWM); LAS unsigned* rr=(LAS unsigned*)(lds+ROWR);
        rm[lane]=a[0]; rr[lane]=a[2]; rm[64+lane]=b[0]; rr[64+lane]=b[2];
        wait_lgkm(); } }
    if constexpr(o.k==K_ABS){
      u32x4 v[5];
      #pragma unroll
      for(int n=0;n<5;n++){ const unsigned col=20*lw+4*n+(lane>>4); v[n]=*(const LAS u32x4*)(lds+(20*lw+4*n<64? ABS1: ABS2-64*256)+col*256+(((lane&15)^(col&15))*16)); }
      #pragma unroll
      for(int n=0;n<5;n++){ const unsigned col=20*lw+4*n+(lane>>4);
        __builtin_amdgcn_raw_buffer_store_b128(v[n],sWk,WS_ABT+((j&7)*80+col)*8192+(j>>3)*4096+pm*256+(lane&15)*16,0,16); } }
    if constexpr(o.k==K_FAB){ TSR(16); if(lw==0 && lane==0) st_flag(p.ws,WS_ABF+((((j>>3)*4+((j&7)>>1))*16+pm)*2+(j&1))*8,tag); }
    if constexpr(o.k==K_PF){
      const char* a;
      if constexpr(o.i<2) a=(const char*)((q<2? p.wsre: p.wsim)+(size_t)(pm*128+32*pn+8*lw+(lane>>3))*S_+(q&1)*1024+((PFU+8*o.i+(lane&7))&15)*64);
      else if constexpr(o.i==2) a=(const char*)p.w2+((size_t)blockIdx.x*64+lane)*120;
      else a=(const char*)p.w1+((size_t)blockIdx.x*64+lane)*120;
      if(o.i<2 || lw==3) asm volatile("global_load_dword %0, %1, off":"+v"(pfv):"v"(a):"memory"); }
    if constexpr(o.k==K_W2L){
      #pragma unroll
      for(int i=0;i<2;i++) asm volatile("buffer_load_dwordx4 %0, %1, %2, 0 offen":"=v"(wv[i]):"v"(wsrc[i]),"s"(sW2s):"memory"); }
    if constexpr(o.k==K_W2S){ wait_vm<behind(K_W2L,K_W2S,NWD)>(); asm volatile("":"+v"(wv[0]),"+v"(wv[1]));
      #pragma unroll
      for(int i=0;i<2;i++) __builtin_amdgcn_raw_buffer_store_b128(wv[i],sWk,wdst[i],0,16); }
    if constexpr(o.k==K_PAB) poll_issue(pv,p.ws,pAB);
    if constexpr(o.k==K_CAB){ wait_vm<behind(K_PAB,K_CAB,NWD)>(); poll_check(pv,p.ws,pAB,tag); TSR(17); }
    if constexpr(o.k==K_HRD){
      #pragma unroll
      for(int rg=0;rg<2;rg++){ const unsigned s=(lw*2+rg)*16+(lane>>2), sw=((s&3)<<2)^(((s>>1)&1)|(((s>>3)&1)<<1));
        #pragma unroll
        for(int G=0;G<3;G++) hv[rg*3+G]=*(const LAS u32x4*)(lds+(lw<2? HST0: HST1-64*256)+s*256+(((4*G+(lane&3))^sw)*16)); }
      wait_lgkm(); }
    if constexpr(o.k==K_HS) __builtin_amdgcn_raw_buffer_store_b128(hv[o.i],sWk,WS_H+(pm*48+3*j+o.i%3)*8192+((lw*2+o.i/3)*16+(lane>>2))*64+(lane&3)*16,0,16);
    if constexpr(o.k==K_FH){ if(lw==0 && lane==0) st_flag(p.ws,WS_HF+(pm*16+j)*8,tag); }
  };
  sfor<TEND+1>([&](auto I){ constexpr int tau=I.value-1, al=allowed(tau,NWD);
    static_assert(al<64,"vmcnt range");
    TW(tau);
    if constexpr(al>=0) wait_vm<al>();
    TX(tau);
    bar();
    TB(tau);
    constexpr int b0=step_begin(tau), b1=step_begin(tau+1);
    sfor<b1-b0>([&](auto M){ exec(ic<b0+M.value>{}); });
  });
  asm volatile(""::"v"(pfv));
  return tag;
}
DI u64 p1_mfma(const Prm& p, LAS char* lds, int pm, int j, unsigned w, unsigned lane TSP){
  u64 tag;
  const unsigned wr=w>>1, wc=w&1, g=lane>>4, c16=lane&15;
  if(w<2){ const unsigned row=pm*128+j*8+w*4+g; u32x4 v[5]; float s=0;
    #pragma unroll
    for(int i=0;i<5;i++) v[i]=*(const u32x4*)(p.x+(size_t)row*D_+(c16+16*i)*8);
    tag=get_tag(p.ws);
    #pragma unroll
    for(int i=0;i<5;i++)
      #pragma unroll
      for(int k=0;k<4;k++) s+=bflo(v[i][k])+bfhi(v[i][k]);
    const float m=sum16(s)*(1.f/D_); float q2=0;
    #pragma unroll
    for(int i=0;i<5;i++)
      #pragma unroll
      for(int k=0;k<4;k++){ const float a=bflo(v[i][k])-m, c=bfhi(v[i][k])-m; q2+=a*a+c*c; }
    const float rs=1.0f/sqrtf(sum16(q2)*(1.f/D_)+1e-5f);
    if(c16<2) st_flag(p.ws,WS_RST+row*16+c16*8, ((u64)(unsigned)tag<<32)|__float_as_uint(c16==0? m: rs));
  } else tag=get_tag(p.ws);
  unsigned cpf=0;
  if constexpr(CPF>0){ if(w==2){ u64 pc; asm volatile("s_getpc_b64 %0":"=s"(pc));
      const char* a=(const char*)(pc&~127ull)+((blockIdx.x>>3)*CPF+(lane<CPF? lane: 0))*128;
      asm volatile("global_load_dword %0, %1, off":"+v"(cpf):"v"(a):"memory"); } }
  TS(1);
  const u32x4 one4={0x3F803F80u,0x3F803F80u,0x3F803F80u,0x3F803F80u}; const bf16x8 ones=__builtin_bit_cast(bf16x8,one4);
  const unsigned rho=3*wr;
  const unsigned lb=(unsigned)(size_t)lds;
  unsigned aA[2][2], aB[2][5], hA[2][2], hB[2][3], aS[5], aC[5], hb[4], hx[4], hs[4];
  #pragma unroll
  for(int s=0;s<2;s++){
    { const unsigned ch=((4*(s^wc)+g)^((lane>>1)&7))*16; aA[0][s]=lb+(32*w+c16)*128+ch; aA[1][s]=aA[0][s]+65536;
      #pragma unroll
      for(int f=0;f<5;f++){ const unsigned fr=(f+rho)%5; aB[s][f]=lb+WS_(0)+(fr*16+c16)*128+ch; } }
    { const unsigned ks=s^wr, ch=((4*ks+g)^((lane>>1)&7))*16; hA[0][s]=lb+(wr*64+c16)*128+ch; hA[1][s]=hA[0][s]+65536;
      #pragma unroll
      for(int f=0;f<3;f++) hB[s][f]=lb+WS_(0)+192*(8*g+(c16>>2))+8*(lane&3)+32*((3*wc+f)^(g&1))+ks*6144; }
  }
  #pragma unroll
  for(int f=0;f<5;f++){ const unsigned fr=(f+rho)%5, col=fr*16+c16;
    aS[f]=lb+(fr<4? ABS1: ABS2-64*256)+col*256+(((4*w+g)^c16)*16); aC[f]=lb+CSX+col*4; }
  #pragma unroll
  for(int fm=0;fm<4;fm++){ const unsigned s=wr*64+32*(fm>>1)+16*(c16>>3)+8*((c16>>2)&1)+4*(fm&1)+(c16&3);
    hs[fm]=lb+ROWM+s*4; hb[fm]=lb+(wr? HST1-64*256: HST0)+s*256+(g&1)*8; hx[fm]=((g>>1)^((s&3)<<2)^(((s>>1)&1)|(((s>>3)&1)<<1)))*16; }
  unsigned aR=lb+ROWM+(32*w+8*g)*4, aH=lb+CSX+(wc*48+4*g)*4;
  #pragma unroll
  for(int s=0;s<2;s++){ PIN(aA[0][s]); PIN(aA[1][s]); PIN(hA[0][s]); PIN(hA[1][s]);
    #pragma unroll
    for(int f=0;f<5;f++) PIN(aB[s][f]);
    #pragma unroll
    for(int f=0;f<3;f++) PIN(hB[s][f]); }
  #pragma unroll
  for(int f=0;f<5;f++){ PIN(aS[f]); PIN(aC[f]); }
  #pragma unroll
  for(int fm=0;fm<4;fm++){ PIN(hs[fm]); PIN(hb[fm]); PIN(hx[fm]); }
  PIN(aR); PIN(aH);
  f32x4 acc[2][5], csa[3];
  #pragma unroll
  for(int k=0;k<5;k++) acc[0][k]=acc[1][k]=(f32x4){0.f,0.f,0.f,0.f};
  csa[0]=csa[1]=csa[2]=(f32x4){0.f,0.f,0.f,0.f};
  bf16x8 fa[2][2], fb[2][5], ga[2][4], gb[2][3]; s16x4 tl[3];
  auto rdA=[&](int m,int i,int s){
    const int xw=xab(i)>=65536, xo=xab(i)-65536*xw, wo=wab(i)-WS_(0);
    if(m==0) fa[s][0]=ldf(at(aA[xw][s],xo)); else if(m<=5) fb[s][m-1]=ldf(at(aB[s][m-1],wo)); else fa[s][1]=ldf(at(aA[xw][s],xo+2048)); };
  auto rdH=[&](int m,int n,int s){
    const int xw=xh(n)>=65536, xo=xh(n)-65536*xw, wo=wh(n)-WS_(0);
    if(m==0) ga[s][0]=ldf(at(hA[xw][s],xo));
    else if(m<=6){ const int c=(m-1)/2; if(m&1) tl[c]=ldtr1(at(hB[s][c],wo)); else gb[s][c]=cat(tl[c],ldtr1(at(hB[s][c],wo+768))); }
    else ga[s][m-6]=ldf(at(hA[xw][s],xo+(m-6)*2048)); };
  if constexpr(CPF>0){ if(w==2) asm volatile("s_waitcnt vmcnt(0)":"+v"(cpf)::"memory"); }
  bar();
  TS(2); TSR(20);
  #pragma unroll
  for(int m=0;m<7;m++) rdA(m,0,0);
  GAP();
  f32x4 m0,m1,r0,r1; float c[5];
  sfor<10>([&](auto T){ constexpr int t=T.value;
    #pragma unroll
    for(int s=0;s<2;s++){
      const bool last=(s==1);
      if(last && t==9){
        #pragma unroll
        for(int f=0;f<3;f++) *(LAS float*)at(aC[f]+wc*320,0)=csa[f][0]; }
      wait_lgkm();
      if(last) bar();
      GAP();
      if(last && t==9){
        #pragma unroll
        for(int f=0;f<5;f++) c[f]=*(const LAS float*)at(aC[f],0)+*(const LAS float*)at(aC[f],320);
        m0=*(const LAS f32x4*)at(aR,0); m1=*(const LAS f32x4*)at(aR,16); r0=*(const LAS f32x4*)at(aR,512); r1=*(const LAS f32x4*)at(aR,528); }
      constexpr int NM= 13;
      #pragma unroll
      for(int m=0;m<NM;m++){
        if(!last){ if(m<3) csa[m]=mma(ones,fb[s][m],csa[m]); else { const int mm=m-3, fm=mm/5, f0=mm%5, fn=fm?4-f0:f0; acc[fm][fn]=mma(fa[s][fm],fb[s][fn],acc[fm][fn]); } }
        else if(m<10){ const int fm=m/5, f0=m%5, fn=fm?4-f0:f0; acc[fm][fn]=mma(fa[s][fm],fb[s][fn],acc[fm][fn]); }
        if(last && t==9){ if(m<10) rdH(m,0,0); }
        else if(m<7) rdA(m,last? t+1: t,s^1);
        GAP();
      }
    }
  });
  TS(3);
  { const float sg= j<8? 1.f: -1.f;
    r0*=sg; r1*=sg;
    #pragma unroll
    for(int f=0;f<5;f++){
      const f32x4 a=(acc[0][f]-m0*c[f])*r0, b=(acc[1][f]-m1*c[f])*r1;
      const u32x4 o={pk(a[0],a[1]),pk(a[2],a[3]),pk(b[0],b[1]),pk(b[2],b[3])};
      *(LAS u32x4*)at(aS[f],0)=o; }
  }
  GAP();
  TS(4); TSR(16);
  f32x4 hac[4][3], csh[3], c4[3];
  #pragma unroll
  for(int k=0;k<3;k++){ csh[k]=(f32x4){0.f,0.f,0.f,0.f};
    #pragma unroll
    for(int i=0;i<4;i++) hac[i][k]=(f32x4){0.f,0.f,0.f,0.f}; }
  float hm[4], hr[4];
  sfor<10>([&](auto T){ constexpr int t=T.value;
    #pragma unroll
    for(int s=0;s<2;s++){
      const bool last=(s==1);
      if(last && t==9){
        #pragma unroll
        for(int f=0;f<3;f++) *(LAS f32x4*)at(aH+wr*384,64*f)=csh[f]; }
      wait_lgkm();
      if(last) bar();
      GAP();
      if(last && t==9){
        #pragma unroll
        for(int f=0;f<3;f++) c4[f]=*(const LAS f32x4*)at(aH,64*f)+*(const LAS f32x4*)at(aH,384+64*f);
        #pragma unroll
        for(int fm=0;fm<4;fm++){ hm[fm]=*(const LAS float*)at(hs[fm],0); hr[fm]=*(const LAS float*)at(hs[fm],512); } }
      #pragma unroll
      for(int m=0;m<15;m++){
        if(!last){ if(m<3) csh[m]=mma(gb[s][m],ones,csh[m]); else { const int mm=m-3, fm=mm/3, f0=mm%3, fn=(fm&1)?2-f0:f0; hac[fm][fn]=mma(gb[s][fn],ga[s][fm],hac[fm][fn]); } }
        else if(m<12){ const int fm=m/3, f0=m%3, fn=(fm&1)?2-f0:f0; hac[fm][fn]=mma(gb[s][fn],ga[s][fm],hac[fm][fn]); }
        if(m<10 && !(last && t==9)) rdH(m,last? t+1: t,s^1);
        GAP();
      }
    }
  });
  TS(5);
  #pragma unroll
  for(int fm=0;fm<4;fm++){ const float zr=hr[fm]*-1.4426950408889634f;
    #pragma unroll
    for(int f=0;f<3;f++){ const f32x4 v=hac[fm][f]-c4[f]*hm[fm], a=v*hr[fm], z=v*zr; f32x4 e;
      #pragma unroll
      for(int k=0;k<4;k++) e[k]=__builtin_amdgcn_exp2f(z[k]);
      e+=1.f;
      #pragma unroll
      for(int k=0;k<4;k++) e[k]=__builtin_amdgcn_rcpf(e[k]);
      const f32x4 h=a*e; const u32x2 o={pk(h[0],h[1]),pk(h[2],h[3])};
      *(LAS u32x2*)at(hb[fm]+(hx[fm]^((3*wc+f)*32)),0)=o; } }
  wait_lgkm();
  bar();
  TS(6); TSR(17);
  return tag;
}

template<class Hook>
DI void y_loop(LAS char* lds, unsigned w, unsigned lane, f32x4 (&acc)[4][5], Hook hook){
  const unsigned wr=w>>1, wc=w&1, g=lane>>4, c16=lane&15;
  unsigned adA[2], adB[2];
  #pragma unroll
  for(int s=0;s<2;s++){ const unsigned ch=((4*s+g)^((lane>>1)&7))*16; adA[s]=(wr*64+c16)*128+ch; adB[s]=(wc*80+c16)*128+ch; }
  #pragma unroll
  for(int i=0;i<4;i++)
    #pragma unroll
    for(int k=0;k<5;k++) acc[i][k]=(f32x4){0.f,0.f,0.f,0.f};
  bf16x8 fa[2][4], fb[2][5];
  auto rd=[&](int m,int t,int ks,int bf){
    const LAS char* bA=lds+slotA(t); const LAS char* bB=lds+slotB(t);
    if(m==0) fa[bf][0]=ldf(bA+adA[ks]);
    else if(m<=5) fb[bf][m-1]=ldf(bB+adB[ks]+(m-1)*2048);
    else fa[bf][m-5]=ldf(bA+adA[ks]+(m-5)*2048); };
  #pragma unroll
  for(int m=0;m<9;m++) rd(m,0,0,0);
  GAP();
  sfor<NY>([&](auto T){ constexpr int t=T.value;
    sfor<2>([&](auto Sx){ constexpr int s=Sx.value; constexpr bool last=(s==1);
      wait_lgkm();
      if(last){ hook(T,ic<-1>{}); bar(); }
      GAP();
      sfor<20>([&](auto M){ constexpr int m=M.value, fm=m/5, f0=m%5, fn=(fm&1)?4-f0:f0;
        acc[fm][fn]=mma(fb[s][fn],fa[s][fm],acc[fm][fn]);
        if(m<9 && !(last && t==NY-1)) rd(m,last? t+1: t,last? 0: 1,s^1);
        hook(T,ic<s*20+m>{});
        GAP();
      });
    });
  });
}
struct GFrag { bf16x8 a[2][2], b[2][3]; };
struct GAddr { unsigned a[2][2], b[3][2]; };
DI GAddr gate_addr(unsigned w, unsigned lane){
  const unsigned wr=w>>1, wc=w&1, g=lane>>4, c16=lane&15; GAddr r;
  #pragma unroll
  for(int ks=0;ks<2;ks++)
    #pragma unroll
    for(int a=0;a<2;a++) r.a[ks][a]=(32*wr+16*(a^wc)+c16)*128+((4*ks+g)^((lane>>1)&7))*16;
  #pragma unroll
  for(int f=0;f<3;f++){ const unsigned b0=160*(8*g+(c16>>2))+8*(lane&3)+32*(wc? 4-f: f); r.b[f][0]=b0+(g&1)*640; r.b[f][1]=b0+640-(g&1)*640; }
  return r; }
DI void gate_rd(int m, const LAS char* lds, const GAddr& ad, int t, int h, GFrag& fr, s16x4 (&tl)[3]){
  const int ks=m/8, i=m%8; const LAS char* bA=lds+slotA(t)+h*8192; const LAS char* bB=lds+slotB(t)+(h*64+ks*32)*160;
  if(i==0) fr.a[ks][0]=ldf(bA+ad.a[ks][0]);
  else if(i<=6){ const int f=(i-1)/2; if(i&1) tl[f]=ldtr1(bB+ad.b[f][0]); else fr.b[ks][f]=cat(tl[f],ldtr1(bB+ad.b[f][1])); }
  else fr.a[ks][1]=ldf(bA+ad.a[ks][1]); }
template<class Hook>
DI void gate_loop(LAS char* lds, const GAddr& ad, GFrag (&fr)[2], f32x4 (&acc)[5], Hook hook){
  s16x4 tl[3];
  #pragma unroll
  for(int k=0;k<5;k++) acc[k]=(f32x4){0.f,0.f,0.f,0.f};
  sfor<NO>([&](auto T){ constexpr int t=NY+T.value;
    sfor<2>([&](auto Sx){ constexpr int s=Sx.value; constexpr bool last=(s==1);
      wait_lgkm();
      if(last){ hook(T,ic<-1>{}); bar(); }
      GAP();
      sfor<10>([&](auto M){ constexpr int m=M.value, ks=m/5, i=m%5, a=(i==2||i==3)? 1: 0, f= i==4? 2: (i&1);
        acc[i]=mma(fr[s].b[ks][f],fr[s].a[ks][a],acc[i]);
        if constexpr(!(last && t==NT-1)){
          if constexpr(m<6){ gate_rd(2*m,lds,ad,last? t+1: t,last? 0: 1,fr[s^1],tl); gate_rd(2*m+1,lds,ad,last? t+1: t,last? 0: 1,fr[s^1],tl); }
          else gate_rd(m+6,lds,ad,last? t+1: t,last? 0: 1,fr[s^1],tl); }
        hook(T,ic<s*10+m>{});
        GAP();
      });
    });
  });
}
struct Qd { u32x4 a,b; u32x2 c; };
DI u32x2 pk4(const f32x4& v){ return (u32x2){pk(v[0],v[1]),pk(v[2],v[3])}; }
template<int W> DI Qd pack_part(const f32x4 (&y)[4][5]){
  constexpr int wr=W>>1, wc=W&1, r0=2*wr+wc, r1=2*wr+1-wc, c0=wc? 4: 0, c1=wc? 3: 1;
  const u32x2 p0=pk4(y[r0][c0]), p1=pk4(y[r0][c1]), p2=pk4(y[r1][c0]), p3=pk4(y[r1][c1]);
  Qd r; r.a=(u32x4){p0[0],p0[1],p1[0],p1[1]}; r.b=(u32x4){p2[0],p2[1],p3[0],p3[1]}; r.c=pk4(y[r0][2]); return r; }
DI void add_part(float (&t)[20], const Qd& r){
  #pragma unroll
  for(int k=0;k<4;k++){ t[2*k]+=bflo(r.a[k]); t[2*k+1]+=bfhi(r.a[k]); t[8+2*k]+=bflo(r.b[k]); t[9+2*k]+=bfhi(r.b[k]); }
  #pragma unroll
  for(int k=0;k<2;k++){ t[16+2*k]+=bflo(r.c[k]); t[17+2*k]+=bfhi(r.c[k]); }
}
#define YSM 1
#define YS0 1
#define YSN 4
#define T_XV 10
#define T_YF 4
#define T_PY 7
#define T_CY 9

DI void p2_mfma(const Prm& p, LAS char* lds, int q, int pm, int pn, unsigned w, unsigned lane, u64 tag TSP){
  const int tile=pm*4+pn;
  const srd_t sY=mksrd(p.ws+WS_YX);
  const unsigned wr=w>>1, wc=w&1, g=lane>>4, c16=lane&15;
  const unsigned sbase=((tile*4+w)*4+q)*4*2560, rbase=(tile*4+q)*16*2560+w*2560;
  const unsigned pY=WS_YF+((tile*4+q)*4+(lane&3))*8;
  u64 pv=0;
  Qd yq[4];
  { f32x4 acc[4][5];
    const unsigned pH= lane<32? WS_HF+(pm*16+(lane&15))*8: WS_ABF+((2*pn+(q&1))*32+(lane&31))*8;
    y_loop(lds,w,lane,acc,[&](auto T,auto M){ constexpr int t=T.value, m=M.value;
      if constexpr(t==T_PH && m==17){ if(w==1) poll_issue(pv,p.ws,pH); }
      if constexpr(t==NY-4 && m==-1){ if(w==1){ wait_vm<0>(); poll_check(pv,p.ws,pH,tag); } } });
    TS(7); TSR(18);
    yq[0]=pack_part<0>(acc); yq[1]=pack_part<1>(acc); yq[2]=pack_part<2>(acc); yq[3]=pack_part<3>(acc); }
  const GAddr ad=gate_addr(w,lane); GFrag fr[2];
  { s16x4 tl[3];
    #pragma unroll
    for(int m=0;m<16;m++) gate_rd(m,lds,ad,NY,0,fr[0],tl); }
  GAP();
  auto ysend=[&](auto K,auto P){ constexpr int k=K.value, pt=P.value;
    if((int)w==q){ LAS char* d=lds+YOWN+k*2560;
      if constexpr(pt==0) *(LAS u32x4*)(d+lane*16)=yq[k].a; else if constexpr(pt==1) *(LAS u32x4*)(d+1024+lane*16)=yq[k].b; else *(LAS u32x2*)(d+2048+lane*8)=yq[k].c;
    } else { const unsigned b=sbase+k*2560;
      if constexpr(pt==0) __builtin_amdgcn_raw_buffer_store_b128(yq[k].a,sY,b+lane*16,0,16);
      else if constexpr(pt==1) __builtin_amdgcn_raw_buffer_store_b128(yq[k].b,sY,b+1024+lane*16,0,16);
      else __builtin_amdgcn_raw_buffer_store_b64(yq[k].c,sY,b+2048+lane*8,0,16); } };
  if constexpr(YSM==0) sfor<12>([&](auto N){ ysend(ic<N.value/3>{},ic<N.value%3>{}); });
  else {
    #pragma unroll
    for(int k=0;k<4;k++) asm volatile("":"+v"(yq[k].a),"+v"(yq[k].b),"+v"(yq[k].c)); }
  GAP();
  TS(8);
  f32x4 og[5]; Qd y[3]; u32x2 xv[5];
  const unsigned row0=pm*128+(q>>1)*64+32*wr+c16+16*wc, row1=row0+16-32*wc, col0=pn*160+(q&1)*80+4*g;
  const unsigned oo[5]={row0*D_+col0+(wc? 64: 0), row0*D_+col0+(wc? 48: 16), row1*D_+col0+(wc? 64: 0), row1*D_+col0+(wc? 48: 16), row0*D_+col0+32};
  auto fetch=[&](auto N){ constexpr int n=N.value;
    if constexpr(n<9){ constexpr int k=n/3, pt=n%3; const unsigned b=rbase+((q+1+k)&3)*4*2560;
      if constexpr(pt==0) y[k].a=__builtin_amdgcn_raw_buffer_load_b128(sY,b+lane*16,0,16);
      else if constexpr(pt==1) y[k].b=__builtin_amdgcn_raw_buffer_load_b128(sY,b+1024+lane*16,0,16);
      else y[k].c=__builtin_amdgcn_raw_buffer_load_b64(sY,b+2048+lane*8,0,16); }
    else if constexpr(n<14) xv[n-9]=*(const u32x2*)(p.x+oo[n-9]); };
  gate_loop(lds,ad,fr,og,[&](auto T,auto M){ constexpr int t=T.value, m=M.value;
    if constexpr(YSM==1 && m>=0){ constexpr int n=(2*t+m/10-YS0)*YSN+(m%10)/2;
      if constexpr(2*t+m/10>=YS0 && (m&1)==1 && (m%10)/2<YSN && n<12) ysend(ic<n/3>{},ic<n%3>{}); }
    if constexpr(t==T_YF && m==-1){ wait_vm<0>(); if(lane==0) st_flag(p.ws,WS_YF+((tile*4+w)*4+q)*8,tag); }
    if constexpr(t==T_PY && m==8) poll_issue(pv,p.ws,pY);
    if constexpr(t==T_XV && m>=11 && m<16) fetch(ic<9+m-11>{});
    if constexpr(t==T_CY && m==-1){ wait_vm<0>(); poll_check(pv,p.ws,pY,tag); }
    if constexpr(m>=0){ constexpr int hh=2*(t-T_CY)+m/10-1;
      if constexpr(hh>=0 && hh<3 && (m%10)%3==1 && (m%10)<9) fetch(ic<3*hh+(m%10)/3>{}); } });
  TS(9); TSR(19);
  { const LAS char* d=lds+YOWN+w*2560; Qd y0; y0.a=*(const LAS u32x4*)(d+lane*16); y0.b=*(const LAS u32x4*)(d+1024+lane*16); y0.c=*(const LAS u32x2*)(d+2048+lane*8);
    float ty[20];
    #pragma unroll
    for(int k=0;k<20;k++) ty[k]=0.f;
    add_part(ty,y0);
    #pragma unroll
    for(int k=0;k<3;k++) add_part(ty,y[k]);
    TS(10);
    #pragma unroll
    for(int k=0;k<5;k++){ u32x2 o;
      #pragma unroll
      for(int e=0;e<2;e++){ const float o0=og[k][2*e], o1=og[k][2*e+1];
        o[e]=pk(bflo(xv[k][e])+ty[4*k+2*e]*(o0*sigm(o0)), bfhi(xv[k][e])+ty[4*k+2*e+1]*(o1*sigm(o1))); }
      *(u32x2*)(p.out+oo[k])=o; }
  }
  TS(11);
}
__global__ __launch_bounds__(512,2) void fnet_mlp_fused(Prm p){
  extern __shared__ __attribute__((aligned(1024))) char smem[];
  LAS char* lds=(LAS char*)smem;
  const unsigned tid=threadIdx.x, lane=tid&63, w=rfl(tid>>6);
  const int b=blockIdx.x, x8=b&7, r=b>>3;
  const int q=x8>>1, pm=(x8&1)*8+(r>>2), pn=r&3, j=q*4+pn;
  TSR(14); TS(0);
  if(w>=4){
    const unsigned lw=w-4;
    const u64 tag= lw<2? loader<3>(p,lds,pm,j,q,pn,lw,lane TSA) : loader<2>(p,lds,pm,j,q,pn,lw,lane TSA);
    TS(8);
    if(b==0 && lw==0){
      #pragma unroll 1
      for(int i=0;i<4;i++) wait_flags(p.ws,WS_ABF+(i*64+lane)*8,tag);
      if(lane==0) st_flag(p.ws,0,tag);
    }
  } else {
    const u64 tag=p1_mfma(p,lds,pm,j,w,lane TSA);
    p2_mfma(p,lds,q,pm,pn,w,lane,tag TSA);
  }
  TSR(15); TSPUT();
}

extern "C" void kernel_launch(void* const* d_in, const int* in_sizes, int n_in, void* d_out, int out_size, void* d_ws, size_t ws_size, hipStream_t stream) {
  Prm p; p.x=(const u16*)d_in[0]; p.wsre=(const u16*)d_in[1]; p.wsim=(const u16*)d_in[2]; p.wdre=(const u16*)d_in[3]; p.wdim=(const u16*)d_in[4];
  p.w1=(const u16*)d_in[6]; p.w2=(const u16*)d_in[7]; p.out=(u16*)d_out; p.ws=(char*)d_ws+WSOFF;
  (void)hipFuncSetAttribute((const void*)fnet_mlp_fused, hipFuncAttributeMaxDynamicSharedMemorySize, LDS_BYTES);
  fnet_mlp_fused<<<256,512,LDS_BYTES,stream>>>(p);
}
```

```cpp
#include <hip/hip_runtime.h>
#include <stdint.h>
#include <stddef.h>
#include <type_traits>
#include <utility>
typedef unsigned short u16;
typedef unsigned long long u64;
typedef float f32x4 __attribute__((ext_vector_type(4)));
typedef float f32x2 __attribute__((ext_vector_type(2)));
typedef __bf16 bf16x8 __attribute__((ext_vector_type(8)));
typedef __bf16 bf16x2 __attribute__((ext_vector_type(2)));
typedef short s16x4 __attribute__((ext_vector_type(4)));
typedef short s16x8 __attribute__((ext_vector_type(8)));
typedef unsigned u32x4 __attribute__((ext_vector_type(4)));
typedef unsigned u32x2 __attribute__((ext_vector_type(2)));
#define DI __device__ __forceinline__
#define GAP() __builtin_amdgcn_sched_barrier(0)
#define LAS __attribute__((address_space(3)))
#define GAS __attribute__((address_space(1)))
typedef GAS u64 gu64;
typedef GAS unsigned gu32;
typedef __amdgpu_buffer_rsrc_t srd_t;

constexpr int S_=2048, D_=640, F_=1536;
constexpr unsigned WS_ABF=8192, WS_HF=12288, WS_YF=16384, WS_RST=65536;
constexpr unsigned WS_ABT=1u<<20, WS_H=8u<<20, WS_YX=16u<<20, WS_W2=30u<<20;
constexpr int XS(int i){ return i*16384; }
constexpr int WS_(int i){ return 98304+i*12288; }
constexpr int ABS1=XS(4), ABS2=155648, AUX=159744, STG=AUX, ROWM=AUX+2048, ROWR=ROWM+512, CSX=ROWM+1024, LDS_BYTES=163840;
constexpr int xab(int i){ return XS(i%6); }
constexpr int wab(int i){ return WS_(i%5); }
constexpr int xh(int n){ return XS((5+n)%6); }
constexpr int WH0=XS(5)+4096;
constexpr int wh(int n){ constexpr int m[10]={0,1,2,3,0,-1,1,2,3,4}; return m[n]<0? WH0: WS_(m[n]); }
constexpr int PA[5]={XS(4),XS(0),XS(1),XS(2),XS(3)}, PB[4]={81920,102400,122880,143360};
constexpr int YOWN=PA[0];
constexpr unsigned SPIN_MAX=40000;
#define WSOFF 0
#define ROTEXPR ((pn+pm)&3)
#define YG 4
#define OROT rot
#define YOFF ((pm+q)&3)
#define OOFF ((pm+q)&3)
#define K0EXPR (pm%10)
constexpr int NY=16, NO=12, NT=NY+NO;
constexpr int slotA(int t){ return t<NY? PA[t%5]: PA[1+(t-NY)%4]; }
constexpr int slotB(int t){ return PB[t&3]; }
constexpr int XOB=slotB(NY+8);

#define TS(i)
#define TB(t)
#define TSR(l)
#define TSRM(l)
#define TBM(t)
#define TSPUT()
#define TW(t)
#define TX(t)
#define TSP
#define TSA

template<int I> using ic=std::integral_constant<int,I>;
template<int... I,class F> DI void sfor_(std::integer_sequence<int,I...>, F&& f){ (f(ic<I>{}),...); }
template<int N,class F> DI void sfor(F&& f){ sfor_(std::make_integer_sequence<int,N>{},f); }
DI unsigned rfl(unsigned x){ return __builtin_amdgcn_readfirstlane(x); }
DI float bflo(unsigned v){ return __uint_as_float(v<<16); }
DI float bfhi(unsigned v){ return __uint_as_float(v&0xffff0000u); }
DI unsigned pk(float lo,float hi){ f32x2 v={lo,hi}; bf16x2 b=__builtin_convertvector(v,bf16x2); return __builtin_bit_cast(unsigned,b); }
DI float sigm(float v){ return __builtin_amdgcn_rcpf(1.f+__builtin_amdgcn_exp2f(-1.4426950408889634f*v)); }
template<int N> DI void wait_vm(){ asm volatile("s_waitcnt vmcnt(%0)"::"n"(N):"memory"); }
DI void bar(){ __builtin_amdgcn_s_barrier(); }
DI void wait_lgkm(){ __builtin_amdgcn_s_waitcnt(0xC07F); }
constexpr unsigned BIAS=4096;
#define DMA1(O,X) "buffer_load_dwordx4 %" #O ", %1, %2 offen" X " lds"
#define DMA_GROUP(X) \
  if constexpr(N==1) asm volatile("s_mov_b32 m0, %0\n\ts_nop 0\n\tbuffer_load_dwordx4 %3, %1, %2 offen" X " lds" :: "s"(m0v),"s"(srd),"s"(soff),"v"(v[0]) : "memory"); \
  if constexpr(N==2) asm volatile("s_mov_b32 m0, %0\n\ts_nop 0\n\tbuffer_load_dwordx4 %3, %1, %2 offen" X " lds\n\tbuffer_load_dwordx4 %4, %1, %2 offen offset:1024" X " lds" :: "s"(m0v),"s"(srd),"s"(soff),"v"(v[0]),"v"(v[1]) : "memory"); \
  if constexpr(N==3) asm volatile("s_mov_b32 m0, %0\n\ts_nop 0\n\tbuffer_load_dwordx4 %3, %1, %2 offen" X " lds\n\tbuffer_load_dwordx4 %4, %1, %2 offen offset:1024" X " lds\n\tbuffer_load_dwordx4 %5, %1, %2 offen offset:2048" X " lds" :: "s"(m0v),"s"(srd),"s"(soff),"v"(v[0]),"v"(v[1]),"v"(v[2]) : "memory"); \
  if constexpr(N==4) asm volatile("s_mov_b32 m0, %0\n\ts_nop 0\n\tbuffer_load_dwordx4 %3, %1, %2 offen" X " lds\n\tbuffer_load_dwordx4 %4, %1, %2 offen offset:1024" X " lds\n\tbuffer_load_dwordx4 %5, %1, %2 offen offset:2048" X " lds\n\tbuffer_load_dwordx4 %6, %1, %2 offen offset:3072" X " lds" :: "s"(m0v),"s"(srd),"s"(soff),"v"(v[0]),"v"(v[1]),"v"(v[2]),"v"(v[3]) : "memory");
#define SC1S " sc1"
template<int N,int POL> DI void dma_group(const __amdgpu_buffer_rsrc_t& srd, const unsigned* v, unsigned soff, unsigned m0v){
  if constexpr(POL==1){ DMA_GROUP(SC1S) } else if constexpr(POL==2){ DMA_GROUP(" nt") } else { DMA_GROUP("") }
}
template<int POL> DI void dma5(const __amdgpu_buffer_rsrc_t& srd, const unsigned* v, unsigned soff, unsigned m0v){
  dma_group<4,POL>(srd,v,soff,m0v); dma_group<1,POL>(srd,v+4,soff,m0v+4096u);
}
#define NTX 0
#define NTD 0
#define NT1 0
#define NTS 0
#define NTSF NTS
#define NTS0 0
#define NT2 0
DI srd_t mksrd(const void* p){ return __builtin_amdgcn_make_buffer_rsrc((void*)p,0,0x7ffffff0,0x00020000); }
DI srd_t mksrd_lo(const void* p){ return mksrd((const char*)p-BIAS); }
DI void st_flag(char* ws, unsigned off, u64 tag){ __hip_atomic_store((gu64*)(ws+off), tag, __ATOMIC_RELAXED, __HIP_MEMORY_SCOPE_AGENT); }
DI void st_f32(char* ws, unsigned off, float v){ __hip_atomic_store((gu32*)(ws+off), __float_as_uint(v), __ATOMIC_RELAXED, __HIP_MEMORY_SCOPE_AGENT); }
DI u64 ld_flag(char* ws, unsigned off){ return __hip_atomic_load((gu64*)(ws+off), __ATOMIC_RELAXED, __HIP_MEMORY_SCOPE_AGENT); }
DI void wait_flags(char* ws, unsigned off, u64 tag){
  #pragma unroll 1
  for(unsigned s=0;s<SPIN_MAX;s++){ const u64 v=ld_flag(ws,off); if(__all(v==tag)) break; __builtin_amdgcn_s_sleep(2); }
}
DI u64 get_tag(const char* ws){ u64 ep; asm volatile("s_load_dwordx2 %0, %1, 0x0\n\ts_waitcnt lgkmcnt(0)":"=s"(ep):"s"(ws):"memory"); return ep+1; }
DI u64 get_tag_out(const char* ws, u16*& out){ u64 ep; const void* ka=(const void*)__builtin_amdgcn_kernarg_segment_ptr();
  asm volatile("s_load_dwordx2 %0, %2, 0x0\n\ts_load_dwordx2 %1, %3, 0x40\n\ts_waitcnt lgkmcnt(0)":"=&s"(ep),"=&s"(out):"s"(ws),"s"(ka):"memory"); return ep+1; }
DI void poll_issue(u64& v, char* ws, unsigned off){ const char* a=ws+off; asm volatile("global_load_dwordx2 %0, %1, off sc1":"=v"(v):"v"(a):"memory"); }
DI void poll_check(u64& v, char* ws, unsigned off, u64 tag){ asm volatile("":"+v"(v)); if(!__all(v==tag)) wait_flags(ws,off,tag); }
DI const LAS char* at(unsigned reg,int off){ return (const LAS char*)(size_t)reg+off; }
#define PIN(x) asm volatile("":"+v"(x))
DI bf16x8 ldf(const LAS char* a){ return *(const LAS bf16x8*)a; }
DI bf16x8 ldtr(const LAS char* a, int step){
  const s16x4 lo=__builtin_amdgcn_ds_read_tr16_b64_v4i16((LAS s16x4*)a), hi=__builtin_amdgcn_ds_read_tr16_b64_v4i16((LAS s16x4*)(a+step));
  return __builtin_bit_cast(bf16x8,(s16x8)__builtin_shufflevector(lo,hi,0,1,2,3,4,5,6,7));
}
DI s16x4 ldtr1(const LAS char* a){ return __builtin_amdgcn_ds_read_tr16_b64_v4i16((LAS s16x4*)a); }
DI bf16x8 cat(s16x4 lo, s16x4 hi){ return __builtin_bit_cast(bf16x8,(s16x8)__builtin_shufflevector(lo,hi,0,1,2,3,4,5,6,7)); }
DI f32x4 mma(bf16x8 a, bf16x8 b, f32x4 c){ return __builtin_amdgcn_mfma_f32_16x16x32_bf16(a,b,c,0,0,0); }

template<int CTRL> DI float dpp_add(float v){ return v+__int_as_float(__builtin_amdgcn_update_dpp(0,__float_as_int(v),CTRL,0xf,0xf,true)); }
DI float sum16(float v){ v=dpp_add<0xB1>(v); v=dpp_add<0x4E>(v); v=dpp_add<0x141>(v); return dpp_add<0x140>(v); }

struct Prm { const u16* x; const u16* wdre; const u16* wdim; char* ws; const u16* w1; const u16* w2; const u16* wsre; const u16* wsim; u16* out; };

enum { K_X, K_WD, K_W1, K_YA, K_YB, K_OA, K_OB, K_STAT, K_STCHK, K_ABS, K_FAB, K_PAB, K_CAB, K_FH, K_PF, K_W2L, K_W2S, K_XO, K_TAG };
struct Op { int k, i, step; };
struct Sched { Op op[240]; int n; };
constexpr int TB1=20, TP2=21, TEND=TP2+NT, TLAST=TEND-2;
#define ST_FAB 13
#define CABM 0
#define T_PA 8
#define ST_ABW 13
#define ST_PAB 17
#define ST_YA0 14
#define ST_YA1 17
#define ST_YA2 19
#define ST_YA3 19
#define ST_TLB 100
#define ST_CAB 18
#define ST_YA1L 100
#define ST_YB1 18
#define ST_YB2 19
#define HRDL 0
#define PFU 4
#define PFY 0
#define ST_PFY 9
#define ST_PFW 100
#define ST_PF1 100
#define ST_W2L 6
#define ST_W2S 9
#define ST_W2LL 100
#define W2SL 0
#define W2H 0
#define ST_FH 2
#define T_PH 10
#define YPAIR 0
#define HS0 0
#define HSN 3
#define HSL 0
#define CPF 10
#define ST_YA4 20
#define T_HA 6
#define HKEEP 9
#define HRE 3
#define TGA 1
#define ST_TAG 2
#define PROE 0
#define WDL 3
#define W15 0
#define ST_W19 11
constexpr Sched make_sched(){
  Sched s{}; auto add=[&](int k,int i,int st){ s.op[s.n].k=k; s.op[s.n].i=i; s.op[s.n].step=st; s.n++; };
#define PRO 1
  if(PRO==0){ add(K_X,0,-2); add(K_WD,0,-2); add(K_X,1,-2); add(K_WD,1,-2); add(K_X,2,-1); add(K_WD,2,-1); add(K_X,3,-1); }
  if(PRO==1){ add(K_X,0,-2); add(K_WD,0,-2); add(K_X,1,-2); add(K_X,2,-2); add(K_WD,1,-2); add(K_WD,2,-1); add(K_X,3,-1); }
  if(PRO==3){ add(K_X,0,-2); add(K_WD,0,-2); add(K_X,1,-2); add(K_WD,1,-2); add(K_X,2,-2); add(K_WD,2,-1); add(K_X,3,-1); }
  if(PRO==4){ add(K_X,0,-2); add(K_WD,0,-2); add(K_WD,1,-2); add(K_X,1,-2); add(K_X,2,-2); add(K_WD,2,-1); add(K_X,3,-1); }
  if(PRO==5){ add(K_X,0,-2); add(K_WD,0,-2); add(K_X,1,-2); add(K_WD,1,-2); add(K_X,2,-2); add(K_WD,2,-2); add(K_X,3,-1); }
  if(WDL==3 && PRO!=2) add(K_WD,3,-1);
  if(PRO==2){ add(K_X,0,-2); add(K_WD,0,-2); add(K_X,1,-2); add(K_WD,1,-2); add(K_WD,2,-2); add(K_X,2,-1); add(K_X,3,-1); }
  for(int st=0;st<20;st++){
    if(st==4) add(K_STAT,0,st);
    if(st==ST_FAB) add(K_FAB,0,st);
    if(st==ST_PAB && !CABM) add(K_PAB,0,st);
    if(WDL==0){ if(st<=6) add(K_WD,st+3,st); }
    else if(WDL==1){ if(st==0) add(K_WD,3,st); if(st<=5) add(K_WD,st+4,st); }
    else if(WDL==2){ if(st==0){ add(K_WD,3,st); add(K_WD,4,st); } if(st<=4) add(K_WD,st+5,st); }
    else if(WDL==3){ if(st<=5) add(K_WD,st+4,st); }
    if(st<=5) add(K_X,st+4,st);
    if(st>=5 && st<=8) add(K_W1,st-5,st);
    if(TGA && st==ST_TAG) add(K_TAG,0,st);
    if(st==ST_W2L) add(K_W2L,0,st);
    if(st==8) add(K_STCHK,0,st);
    if(st==ST_W2S && !W2SL) add(K_W2S,0,st);
    if(st==ST_PFY && PFY>0) add(K_PF,0,st);
    if(st==ST_PFY && PFY>1) add(K_PF,1,st);
    if(st==ST_PFW) add(K_PF,2,st);
    if(st==ST_PF1) add(K_PF,3,st);
    if(st==ST_TLB) add(K_PF,4,st);
    if(st==10){ add(K_W1,4,st); if(W15==0) add(K_W1,5,st); add(K_ABS,0,st); if(W15==1) add(K_W1,5,st); }
    if(st==11 && W15==2) add(K_W1,5,st);
    if(st>=11 && st<=13) add(K_W1,st-5,st);
    if(st==ST_W19) add(K_W1,9,st);
    if(st==ST_YA0) add(K_YA,0,st);
    if(st==ST_YA1) add(K_YA,1,st);
    if(st==ST_CAB){ if(!CABM) add(K_CAB,0,st); add(K_YB,0,st); }
    if(st==ST_YB1) add(K_YB,1,st);
    if(st==ST_YA1L) add(K_YA,1,st);
    if(st==19){ if(ST_YA2==19) add(K_YA,2,st); if(ST_YB2==19) add(K_YB,2,st); if(ST_YA3==19) add(K_YA,3,st); }
    if(st==ST_YA2 && st!=19) add(K_YA,2,st);
    if(st==ST_YA3 && st!=19) add(K_YA,3,st);
    if(st==ST_YB2 && st!=19) add(K_YB,2,st);
    if(st==ST_YA4) add(K_YA,4,st);
    if(st==ST_W2LL) add(K_W2L,0,st);
    if(st==ST_W2S && W2SL) add(K_W2S,0,st);
  }
  if(ST_YB1==20) add(K_YB,1,TB1);
  if(ST_YA2==20) add(K_YA,2,TB1);
  if(ST_YB2==20) add(K_YB,2,TB1);
  if(ST_YA3==20) add(K_YA,3,TB1);
  if(ST_YA4==20) add(K_YA,4,TB1);
  add(K_YB,3,TB1);
  for(int t=0;t<NT;t++){ const int st=TP2+t;
    if(t==ST_FH) add(K_FH,0,st);
    if(YPAIR==0){
      if(t+4<NY) add(K_YB,t+4,st);
      if(t+5<NY) add(K_YA,t+5,st); }
    if(YPAIR==1){
      if(t+4<NY) add(K_YB,t+4,st);
      if(t==0) add(K_YA,5,st);
      if(t>0 && (t&1)==0){ if(t+4<NY) add(K_YA,t+4,st); if(t+5<NY) add(K_YA,t+5,st); } }
    if(YPAIR==2){
      if(t==0) add(K_YA,5,st);
      if(t>0 && (t&1)==0){ if(t+4<NY) add(K_YA,t+4,st); if(t+5<NY) add(K_YA,t+5,st); }
      if(t+4<NY) add(K_YB,t+4,st); }
    if(t+4>=NY && t+4<NT){ add(K_OA,t+4,st); add(K_OB,t+4,st); }
    if(t==NY+8) add(K_XO,0,st);
  }
  return s;
}
constexpr Sched SCH=make_sched();
constexpr int op_cnt(const Op& o,int nwd){
  return o.k==K_WD? nwd: (o.k==K_W1||o.k==K_XO)? 3: (o.k==K_X||o.k==K_YA||o.k==K_OA)? 4: (o.k==K_YB||o.k==K_OB||o.k==K_ABS)? 5: (o.k==K_W2L||o.k==K_W2S)? 2: (o.k==K_PAB||(o.k==K_PF && (o.i<2||o.i==4)))? 1: 0; }
constexpr int first_use(const Op& o){
  return (o.k==K_X||o.k==K_WD)? o.i-1: o.k==K_W1? o.i+9: (o.k==K_YA||o.k==K_YB||o.k==K_OA||o.k==K_OB)? TB1+o.i: o.k==K_STAT? 8: o.k==K_ABS? ST_ABW: o.k==K_XO? TLAST: 1000; }
constexpr int last_use(const Op& o){
  return o.k==K_X? (o.i>=5? o.i+5: o.i): o.k==K_WD? o.i: o.k==K_W1? o.i+10: (o.k==K_YA||o.k==K_YB||o.k==K_OA||o.k==K_OB)? TP2+o.i: o.k==K_STAT? 9: o.k==K_XO? 1000: -1000; }
constexpr int lds_lo(const Op& o){
  return o.k==K_X? xab(o.i): o.k==K_WD? wab(o.i): o.k==K_W1? wh(o.i): (o.k==K_YA||o.k==K_OA)? slotA(o.i): (o.k==K_YB||o.k==K_OB)? slotB(o.i): o.k==K_STAT? STG: o.k==K_XO? XOB: -1; }
constexpr int lds_sz(const Op& o){
  return o.k==K_WD? 10240: o.k==K_W1? 12288: (o.k==K_X||o.k==K_YA||o.k==K_OA)? 16384: (o.k==K_YB||o.k==K_OB)? 20480: o.k==K_STAT? 2048: o.k==K_XO? 10240: 0; }
constexpr bool needed(const Op& o,int tau){ return first_use(o)==tau; }
constexpr int last_needed(int tau){ int l=-1; for(int n=0;n<SCH.n;n++) if(SCH.op[n].step<tau && needed(SCH.op[n],tau)) l=n; return l; }
constexpr int allowed(int tau,int nwd){ const int l=last_needed(tau); if(l<0) return -1; int c=0; for(int n=l+1;n<SCH.n;n++) if(SCH.op[n].step<tau) c+=op_cnt(SCH.op[n],nwd); return c; }
constexpr int find_op(int k,int i){ for(int n=0;n<SCH.n;n++) if(SCH.op[n].k==k && SCH.op[n].i==i) return n; return 1<<20; }
constexpr bool done_by(int k,int i,int tau){ const int p=find_op(k,i); for(int t=-1;t<=tau;t++) if(last_needed(t)>=p) return true; return false; }
constexpr int behind(int k0,int k1,int nwd){ int c=0; for(int n=find_op(k0,0)+1;n<find_op(k1,0);n++) c+=op_cnt(SCH.op[n],nwd); return c; }
constexpr int step_begin(int st){ int n=0; while(n<SCH.n && SCH.op[n].step<st) n++; return n; }
struct Reg { int lo, sz, from, to; };
constexpr Reg STAGED[5]={ {ABS1,16384,9,11}, {ABS2,4096,9,11}, {YOWN,10240,TP2+NY-1,1000},
                          {ROWM,1024,8,TB1}, {CSX,1024,7,TB1} };
constexpr bool sched_ok(){
  for(int n=0;n<SCH.n;n++){ const Op& a=SCH.op[n];
    if(n && a.step<SCH.op[n-1].step) return false;
    if(lds_sz(a)==0) continue;
    if(a.step>=first_use(a)) return false;
    for(int m=0;m<SCH.n;m++){ const Op& b=SCH.op[m]; if(m==n || lds_sz(b)==0) continue;
      if(lds_lo(a)<lds_lo(b)+lds_sz(b) && lds_lo(b)<lds_lo(a)+lds_sz(a) && a.step<last_use(b) && b.step<last_use(a)) return false; }
    for(const Reg& r: STAGED) if(lds_lo(a)<r.lo+r.sz && r.lo<lds_lo(a)+lds_sz(a) && a.step<r.to && r.from<last_use(a)) return false; }
  for(int k: {K_X,K_WD,K_W1}) for(int i=0;i<10;i++) if(find_op(k,i)>=SCH.n) return false;
  for(int i=0;i<NT;i++) if(find_op(i<NY? K_YA: K_OA,i)>=SCH.n || find_op(i<NY? K_YB: K_OB,i)>=SCH.n) return false;
  return true; }
static_assert(offsetof(Prm,out)==0x40 && sizeof(Prm)==72,"kernel argument layout");
static_assert(sched_ok(),"LDS regions are reused only behind the barrier that ends their last read");
static_assert(find_op(K_W2L,0)<find_op(K_W2S,0) && (W2H? done_by(K_W2S,0,TP2+ST_FH): find_op(K_W2S,0)<find_op(K_ABS,0)),"the a/b flag or the h flag also covers the re-laid part of w2");
static_assert(done_by(K_ABS,0,ST_FAB),"a flag follows its completed stores");
static_assert(CABM? SCH.op[find_op(K_YB,0)].step>=19: (find_op(K_CAB,0)<find_op(K_YB,0) && find_op(K_PAB,0)<find_op(K_CAB,0)),"a/b tiles are loaded behind the check of their flags");
static_assert(T_PH<NY-4 && SCH.op[find_op(K_OA,NY)].step>=TP2+NY-4,"h tiles are loaded behind the check of their flags");

constexpr int NWD=3;
DI void dma_wd(const __amdgpu_buffer_rsrc_t& srd, const unsigned* v, unsigned soff, unsigned m0v){
  asm volatile("s_mov_b32 m0, %0\n\ts_nop 0\n\tbuffer_load_dwordx4 %3, %1, %2 offen lds\n\tbuffer_load_dwordx4 %4, %1, %2 offen offset:1024 lds\n\t"
               "s_mov_b32 exec_hi, 0\n\tbuffer_load_dwordx4 %5, %1, %2 offen offset:2048 lds\n\ts_mov_b32 exec_hi, -1"
               :: "s"(m0v),"s"(srd),"s"(soff),"v"(v[0]),"v"(v[1]),"v"(v[2]) : "memory"); }
DI u64 loader(const Prm& p, LAS char* lds, int pm, int j, int q, int pn, unsigned lw, unsigned lane TSP){
  const unsigned ldsb=(unsigned)(size_t)lds;
  const unsigned r=lane>>3;
  const int rot=ROTEXPR, k0=K0EXPR, orot=OROT, yoff=YOFF, ooff=OOFF;
  u64 tag, pv=0;
  unsigned vX[4], vD[NWD], vW[3];
  #pragma unroll
  for(int i=0;i<4;i++) vX[i]=(32*lw+16*(i&1)+4*(i>>1)+8*(r>>2)+(r&3))*(D_*2)+((lane&7)^((i&1)*4+(r>>1)))*16+BIAS-1024*i;
  #pragma unroll
  for(int i=0;i<NWD;i++) vD[i]=(20*lw+8*i+r)*(D_*2)+((lane&7)^((2*lw+4*i+(r>>1))&7))*16+BIAS-1024*i;
  auto ktile=[&](int i){ const int kt=k0+i; return kt>=10? kt-10: kt; };
  unsigned ko[3]={ktile(0)*128u,ktile(1)*128u,ktile(2)*128u}, mX=ldsb+lw*4096u, mD=ldsb+lw*2560u, oA=pm*128*D_*2-BIAS, oD=(j&7)*80*D_*2-BIAS;
  #pragma unroll
  for(int i=0;i<4;i++) PIN(vX[i]);
  #pragma unroll
  for(int i=0;i<NWD;i++) PIN(vD[i]);
  asm volatile("":"+s"(ko[0]),"+s"(ko[1]),"+s"(ko[2]),"+s"(mX),"+s"(mD),"+s"(oA),"+s"(oD));
  GAP();
  const srd_t sA=mksrd((const char*)p.x+(int)oA);
  auto kofs=[&](int i){ return i<3? ko[i]: ktile(i)*128u; };
  auto ldx=[&](int i,int dst){ dma_group<4,NTX>(sA,vX,kofs(i),mX+dst); };
  TS(1);
  u64 ep;
  if constexpr(TGA) asm volatile("s_load_dwordx2 %0, %1, 0x0":"=s"(ep):"s"(p.ws):"memory");
  ldx(0,xab(0));
  GAP();
  const srd_t sD=mksrd((const char*)(j<8? p.wdre: p.wdim)+(int)oD);
  auto ldwd=[&](int i){ dma_wd(sD,vD,kofs(i),mD+wab(i)); };
  constexpr int NPRE=step_begin(-1);
  static_assert(SCH.op[0].k==K_X && SCH.op[0].i==0 && SCH.op[1].k==K_WD && SCH.op[1].i==0,"prologue");
  ldwd(0);
  auto prologue=[&](){ sfor<NPRE-2>([&](auto N){ constexpr Op o=SCH.op[N.value+2]; static_assert(o.k==K_X||o.k==K_WD,"prologue");
    if constexpr(o.k==K_X) ldx(o.i,xab(o.i)); else ldwd(o.i); }); };
  if constexpr(PROE) prologue();
  GAP();
  TS(2);
  const srd_t sW=mksrd_lo(p.w1+j*96);
  #pragma unroll
  for(int i=0;i<3;i++){ const unsigned c=i*64+lane, row=c/12, ch=c%12; vW[i]=(16*lw+row)*(F_*2)+32*((ch>>1)^((row>>3)&1))+16*(ch&1)+BIAS-1024*i; }
  if constexpr(!TGA) tag=get_tag(p.ws);
  const srd_t sWs=mksrd_lo((q<2? p.wsre: p.wsim)+(size_t)pm*128*S_+(q&1)*1024), sT=mksrd_lo(p.ws+WS_ABT+(size_t)pn*160*8192+q*2048);
  const srd_t sH=mksrd_lo(p.ws+WS_H+(size_t)pm*48*8192+(q>>1)*1024), sW2=mksrd_lo(p.ws+WS_W2+(2*pn+(q&1))*245760), sWk=mksrd(p.ws);
  unsigned vA[4], vB[5], vH[4], vW2[5];
  #pragma unroll
  for(int i=0;i<4;i++){ const unsigned c=(lane&7)^((i&1)*4+(r>>1));
    vA[i]=(32*lw+8*i+r)*(S_*2)+c*16+BIAS-1024*i;
    vH[i]=lw*8192+i*2048+(32*(lane>>5)+16*((lane>>2)&1)+4*((lane>>3)&3)+(lane&3))*16+BIAS-1024*i; }
  #pragma unroll
  for(int i=0;i<5;i++){ const unsigned bi=BIAS-(i<4? 1024*i: 0);
    vB[i]=((5*lw+i)*8+r)*8192+((lane&7)^(((lw+i)&1)*4+(r>>1)))*16+bi;
    vW2[i]=lw*5120+lane*16+BIAS+(i<4? 0: 4096); }
  u32x4 wv[2]; unsigned wsrc[2], wdst[2];
  #pragma unroll
  for(int i=0;i<2;i++){ const unsigned cc=i*60+(lane<60? lane: 59), rho=(2*pm+(j&1))*48+12*lw+cc/10, ch=cc%10, kk=rho^(((rho>>3)&1)<<2),
      row=(rho&~31u)+((kk>>2)&1)*16+((kk>>3)&3)*4+(kk&3), sl=j>>1;
    wsrc[i]=row*(D_*2)+((sl>>1)*160+(sl&1)*80)*2+ch*16; wdst[i]=WS_W2+sl*245760+rho*160+ch*16; }
  const srd_t sW2s=mksrd(p.w2);
  const unsigned lA2=ldsb+lw*4096u, lB2=ldsb+lw*5120u;
  unsigned pfv=0;
  unsigned pAB=WS_ABF+((((q>>1)*4+pn)*16+(q&1)*8)*2+(lane&15))*8;
  #pragma unroll
  for(int i=0;i<4;i++){ PIN(vA[i]); PIN(vH[i]); }
  #pragma unroll
  for(int i=0;i<5;i++){ PIN(vB[i]); PIN(vW2[i]); }
  #pragma unroll
  for(int i=0;i<3;i++) PIN(vW[i]);
  PIN(wsrc[0]); PIN(wsrc[1]); PIN(wdst[0]); PIN(wdst[1]); PIN(pAB);
  GAP();
  TS(3);
  if constexpr(!PROE) prologue();
  TS(4);
  auto exec=[&](auto N){ constexpr Op o=SCH.op[N.value];
    if constexpr(o.k==K_X) ldx(o.i,xab(o.i));
    if constexpr(o.k==K_WD) ldwd(o.i);
    if constexpr(o.k==K_W1) dma_group<3,NT1>(sW,vW,ktile(o.i>=5? o.i-5: o.i+5)*(64u*F_*2),ldsb+lw*3072u+wh(o.i));
    if constexpr(o.k==K_YA){ const int i2=(o.i+yoff)&15, u=(i2&~(YG-1))|((i2+rot)&(YG-1));
      if constexpr(NTS0>o.i) dma_group<4,2>(sWs,vA,u*128u,lA2+slotA(o.i));
      else if constexpr(NTSF!=NTS){ if(o.i<=(YPAIR?1:0) || (i2&(YG-1))<=(YPAIR?1:0)) dma_group<4,NTSF>(sWs,vA,u*128u,lA2+slotA(o.i)); else dma_group<4,NTS>(sWs,vA,u*128u,lA2+slotA(o.i)); }
      else dma_group<4,NTS>(sWs,vA,u*128u,lA2+slotA(o.i)); }
    if constexpr(o.k==K_YB){ const int i2=(o.i+yoff)&15, u=(i2&~(YG-1))|((i2+rot)&(YG-1)); dma5<1>(sT,vB,u*128u,lB2+slotB(o.i)); }
    if constexpr(o.k==K_OA){ int i2=o.i-NY+ooff; i2= i2>=NO? i2-NO: i2; const int u=(i2&~3)|((i2+orot)&3); dma_group<4,1>(sH,vH,u*32768u,lA2+slotA(o.i)); }
    if constexpr(o.k==K_OB){ int i2=o.i-NY+ooff; i2= i2>=NO? i2-NO: i2; const int u=(i2&~3)|((i2+orot)&3); dma5<1>(sW2,vW2,u*20480u,lB2+slotB(o.i)); }
    if constexpr(o.k==K_TAG){ asm volatile("s_waitcnt lgkmcnt(0)":"+s"(ep)::"memory"); tag=ep+1; }
    if constexpr(o.k==K_STAT){ if(lw==0){ const unsigned v0=WS_RST+pm*2048+lane*16, vs[2]={v0,v0}; dma_group<2,1>(sWk,vs,0,ldsb+STG); } }
    if constexpr(o.k==K_STCHK){ if(lw==0){
        u32x4 a=*(const LAS u32x4*)(lds+STG+lane*16), b=*(const LAS u32x4*)(lds+STG+1024+lane*16);
        const unsigned tg=(unsigned)tag;
        if(!__all(a[1]==tg && a[3]==tg && b[1]==tg && b[3]==tg)){
          #pragma unroll 1
          for(unsigned sp=0;sp<SPIN_MAX;sp++){ asm volatile("":::"memory");
            a=__builtin_amdgcn_raw_buffer_load_b128(sWk,WS_RST+pm*2048+lane*16,0,16); b=__builtin_amdgcn_raw_buffer_load_b128(sWk,WS_RST+pm*2048+1024+lane*16,0,16);
            if(__all(a[1]==tg && a[3]==tg && b[1]==tg && b[3]==tg)) break; } }
        LAS unsigned* rm=(LAS unsigned*)(lds+ROWM); LAS unsigned* rr=(LAS unsigned*)(lds+ROWR);
        rm[lane]=a[0]; rr[lane]=a[2]; rm[64+lane]=b[0]; rr[64+lane]=b[2];
        wait_lgkm(); } }
    if constexpr(o.k==K_ABS){
      u32x4 v[5];
      #pragma unroll
      for(int n=0;n<5;n++){ const unsigned col=20*lw+4*n+(lane>>4); v[n]=*(const LAS u32x4*)(lds+(20*lw+4*n<64? ABS1: ABS2-64*256)+col*256+(((lane&15)^(col&15))*16)); }
      #pragma unroll
      for(int n=0;n<5;n++){ const unsigned col=20*lw+4*n+(lane>>4);
        __builtin_amdgcn_raw_buffer_store_b128(v[n],sWk,WS_ABT+((j&7)*80+col)*8192+(j>>3)*4096+pm*256+(lane&15)*16,0,16); } }
    if constexpr(o.k==K_FAB){ TSR(16); if(lw==0 && lane==0) st_flag(p.ws,WS_ABF+((((j>>3)*4+((j&7)>>1))*16+pm)*2+(j&1))*8,tag); }
    if constexpr(o.k==K_PF){
      const char* a;
      if constexpr(o.i<2) a=(const char*)((q<2? p.wsre: p.wsim)+(size_t)(pm*128+32*pn+8*lw+(lane>>3))*S_+(q&1)*1024+((PFU+8*o.i+(lane&7))&15)*64);
      else if constexpr(o.i==2) a=(const char*)p.w2+((size_t)blockIdx.x*64+lane)*120;
      else if constexpr(o.i==3) a=(const char*)p.w1+((size_t)blockIdx.x*64+lane)*120;
      else a=(const char*)((q<2? p.wsre: p.wsim)+(size_t)(pm*128+32*lw)*S_+(q&1)*1024);
      if(o.i<2 || o.i==4 || lw==3) asm volatile("global_load_dword %0, %1, off":"+v"(pfv):"v"(a):"memory"); }
    if constexpr(o.k==K_W2L){
      #pragma unroll
      for(int i=0;i<2;i++) asm volatile("buffer_load_dwordx4 %0, %1, %2, 0 offen":"=v"(wv[i]):"v"(wsrc[i]),"s"(sW2s):"memory"); }
    if constexpr(o.k==K_W2S){ wait_vm<behind(K_W2L,K_W2S,NWD)>(); asm volatile("":"+v"(wv[0]),"+v"(wv[1]));
      #pragma unroll
      for(int i=0;i<2;i++) __builtin_amdgcn_raw_buffer_store_b128(wv[i],sWk,wdst[i],0,16); }
    if constexpr(o.k==K_XO){
      const srd_t sXo=mksrd_lo(p.x+(size_t)(pm*128+(q>>1)*64+16*lw)*D_+pn*160+(q&1)*80); unsigned vo[3];
      #pragma unroll
      for(int i=0;i<3;i++){ const unsigned c=64*i+(i<2? lane: lane&31); vo[i]=(c/10)*(D_*2)+(c%10)*16+BIAS-1024*i; }
      dma_wd(sXo,vo,0,ldsb+lw*2560u+XOB); }
    if constexpr(o.k==K_PAB){ TSR(21); poll_issue(pv,p.ws,pAB); }
    if constexpr(o.k==K_CAB){ TSR(22); wait_vm<behind(K_PAB,K_CAB,NWD)>(); TSR(23);
      poll_check(pv,p.ws,pAB,tag); TSR(17); }
    if constexpr(o.k==K_FH){ if(lw==0 && lane==0) st_flag(p.ws,WS_HF+(pm*16+j)*8,tag); }
  };
  sfor<TLAST+2>([&](auto I){ constexpr int tau=I.value-1, al=allowed(tau,NWD);
    static_assert(al<64,"vmcnt range");
    TW(tau);
    if constexpr(al>=0) wait_vm<al>();
    TX(tau);
    bar();
    TB(tau);
    constexpr int b0=step_begin(tau), b1=step_begin(tau+1);
    sfor<b1-b0>([&](auto M){ exec(ic<b0+M.value>{}); });
  });
  asm volatile(""::"v"(pfv));
  return tag;
}
template<class Hook>
DI u64 p1_mfma(const Prm& p, LAS char* lds, int pm, int j, unsigned w, unsigned lane, unsigned& cpf, u32x4 (&v)[5], u16*& out, Hook hook TSP){
  u64 tag;
  const unsigned wr=w>>1, wc=w&1, g=lane>>4, c16=lane&15;
  u32x2 tv;
  if constexpr(TGA){ const void* ka=(const void*)__builtin_amdgcn_kernarg_segment_ptr(); unsigned z=0; PIN(z);
    asm volatile("s_load_dwordx2 %0, %2, 0x40\n\tglobal_load_dwordx2 %1, %3, %4":"=&s"(out),"=&v"(tv):"s"(ka),"v"(z),"s"(p.ws):"memory"); }
  else tag=get_tag_out(p.ws,out);
  TS(1);
  const u32x4 one4={0x3F803F80u,0x3F803F80u,0x3F803F80u,0x3F803F80u}; const bf16x8 ones=__builtin_bit_cast(bf16x8,one4);
  const unsigned rho=3*wr;
  const unsigned lb=(unsigned)(size_t)lds;
  unsigned aA[2][2], aB[2][5], hB[2][2], aS[5], aC[5], hs[2], aH[2], hG[3];
  #pragma unroll
  for(int s=0;s<2;s++){
    { const unsigned ch=((4*(s^wc)+g)^((lane>>1)&7))*16; aA[0][s]=lb+(32*w+c16)*128+ch; aA[1][s]=aA[0][s]+65536;
      #pragma unroll
      for(int f=0;f<5;f++){ const unsigned fr=(f+rho)%5; aB[s][f]=lb+WS_(0)+(fr*16+c16)*128+ch; } }
    #pragma unroll
    for(int e=0;e<2;e++) hB[s][e]=lb+WH0+192*(8*g+(c16>>2))+8*(lane&3)+32*(e^wr^(g&1))+(s^wc)*6144;
  }
  #pragma unroll
  for(int f=0;f<5;f++){ const unsigned fr=(f+rho)%5, col=fr*16+c16;
    aS[f]=lb+(fr<4? ABS1: ABS2-64*256)+col*256+(((4*w+g)^c16)*16); aC[f]=lb+CSX+col*4; }
  auto h_addr=[&](){
    #pragma unroll
    for(int fm=0;fm<2;fm++){ const unsigned s=32*w+16*(c16>>3)+8*((c16>>2)&1)+4*fm+(c16&3);
      hs[fm]=lb+ROWM+s*4; aH[fm]=lb+CSX+16*g+64*(fm^wr);
      PIN(hs[fm]); PIN(aH[fm]); }
    #pragma unroll
    for(int P=0;P<3;P++){ hG[P]=P*8192+g*2048+(32*w+c16)*16; PIN(hG[P]); } };
  unsigned aR=lb+ROWM+(32*w+8*g)*4;
  #pragma unroll
  for(int s=0;s<2;s++){ PIN(aA[0][s]); PIN(aA[1][s]); PIN(hB[s][0]); PIN(hB[s][1]);
    #pragma unroll
    for(int f=0;f<5;f++) PIN(aB[s][f]); }
  #pragma unroll
  for(int f=0;f<5;f++){ PIN(aS[f]); PIN(aC[f]); }
  PIN(aR);
  if constexpr(T_HA<0) h_addr();
  f32x4 acc[2][5], csa[3];
  #pragma unroll
  for(int k=0;k<5;k++) acc[0][k]=acc[1][k]=(f32x4){0.f,0.f,0.f,0.f};
  csa[0]=csa[1]=csa[2]=(f32x4){0.f,0.f,0.f,0.f};
  bf16x8 xr[5][2][2], fa[2][2], fb[2][5], gb[2][6]; s16x4 tl[6];
  auto rdA=[&](int m,int i,int s){
    const int xw=xab(i)>=65536, xo=xab(i)-65536*xw, wo=wab(i)-WS_(0);
    if(m==0) (i<5? xr[i<5? i: 0][s][0]: fa[s][0])=ldf(at(aA[xw][s],xo));
    else if(m<=5) fb[s][m-1]=ldf(at(aB[s][m-1],wo));
    else (i<5? xr[i<5? i: 0][s][1]: fa[s][1])=ldf(at(aA[xw][s],xo+2048)); };
  auto rdH=[&](int k,int n,int s){
    const int xw=xh(n)>=65536, xo=xh(n)-65536*xw, wo=wh(n)-WH0;
    if(n<5 && k==0) fa[s][0]=ldf(at(aA[xw][s],xo));
    else if(n<5 && k==5) fa[s][1]=ldf(at(aA[xw][s],xo+2048));
    else { const int b= n<5? k-1-(k>5): k, c=b/2;
      if(!(b&1)) tl[c]=ldtr1(at(hB[s][c&1],wo+64*(c>>1))); else gb[s][c]=cat(tl[c],ldtr1(at(hB[s][c&1],wo+64*(c>>1)+768))); } };
  auto rdHs=[&](int m,int nm,int n,int s){
    const int R= n<5? 14: 12, E= nm<R? nm: R, D=R-E;
    if(m<D){ rdH(2*m,n,s); rdH(2*m+1,n,s); } else if(m<E) rdH(D+m,n,s); };
  auto stats=[&](){ if(w<2){ const unsigned row=pm*128+j*8+w*4+g;
    asm volatile("s_waitcnt vmcnt(0)":"+v"(cpf),"+v"(v[0]),"+v"(v[1]),"+v"(v[2]),"+v"(v[3]),"+v"(v[4])::"memory");
    if constexpr(TGA) asm volatile("":"+v"(tv));
    const bf16x2 one2=__builtin_bit_cast(bf16x2,0x3F803F80u);
    float s[2]={0.f,0.f}, q2[2]={0.f,0.f};
    #pragma unroll
    for(int i=0;i<5;i++)
      #pragma unroll
      for(int k=0;k<4;k++){ const unsigned u=v[i][k]; const bf16x2 a=__builtin_bit_cast(bf16x2,u);
        s[k&1]=__builtin_amdgcn_fdot2_f32_bf16(a,one2,s[k&1],false); q2[k&1]=__builtin_amdgcn_fdot2_f32_bf16(a,a,q2[k&1],false); }
    const float m=sum16(s[0]+s[1])*(1.f/D_), var=sum16(q2[0]+q2[1])*(1.f/D_)-m*m;
    const float rs=__builtin_amdgcn_rsqf(fmaxf(var,0.f)+1e-5f);
    if(c16<2) st_flag(p.ws,WS_RST+row*16+c16*8, ((u64)(TGA? tv[0]+1u: (unsigned)tag)<<32)|__float_as_uint(c16==0? m: rs));
  } };
#define STPOS 1
  if constexpr(STPOS==0) stats();
  if constexpr(TGA) asm volatile("s_waitcnt lgkmcnt(0)":"+s"(out)::"memory");
  bar();
  TBM(-1);
  TS(2); TSR(20);
  #pragma unroll
  for(int m=0;m<7;m++) rdA(m,0,0);
  GAP();
  if constexpr(STPOS==1){ stats(); GAP(); }
  f32x4 m0,m1,r0,r1; float c0[5], c1[5];
  sfor<10>([&](auto T){ constexpr int t=T.value;
    #pragma unroll
    for(int s=0;s<2;s++){
      const bool last=(s==1);
      if(TGA && last && t==1){ asm volatile("s_waitcnt vmcnt(0)":"+v"(tv)::"memory"); tag=(((u64)rfl(tv[1])<<32)|rfl(tv[0]))+1; }
      if(last && t==9){
        #pragma unroll
        for(int f=0;f<3;f++) *(LAS float*)at(aC[f]+wc*320,0)=csa[f][0]; }
      wait_lgkm();
      if(last){ bar(); TBM(t); }
      GAP();
      if(last && t==9){
        #pragma unroll
        for(int f=0;f<5;f++){ c0[f]=*(const LAS float*)at(aC[f],0); c1[f]=*(const LAS float*)at(aC[f],320); }
        m0=*(const LAS f32x4*)at(aR,0); m1=*(const LAS f32x4*)at(aR,16); r0=*(const LAS f32x4*)at(aR,512); r1=*(const LAS f32x4*)at(aR,528); }
      constexpr int NM= 13;
      #pragma unroll
      for(int m=0;m<NM;m++){
        const bf16x8 (&xa)[2]= t<5? xr[t<5? t: 0][s]: fa[s];
        if(!last){ if(m<3) csa[m]=mma(ones,fb[s][m],csa[m]); else { const int mm=m-3, fm=mm/5, f0=mm%5, fn=fm?4-f0:f0; acc[fm][fn]=mma(xa[fm],fb[s][fn],acc[fm][fn]); } }
        else if(m<10){ const int fm=m/5, f0=m%5, fn=fm?4-f0:f0; acc[fm][fn]=mma(xa[fm],fb[s][fn],acc[fm][fn]); }
        if(last && t==9){ rdHs(m,10,0,0); }
        else if(m<7) rdA(m,last? t+1: t,s^1);
        GAP();
      }
    }
  });
  TS(3);
  { const float sg= j<8? 1.f: -1.f;
    r0*=sg; r1*=sg;
    #pragma unroll
    for(int f=0;f<5;f++){
      const float c=c0[f]+c1[f]; const f32x4 a=(acc[0][f]-m0*c)*r0, b=(acc[1][f]-m1*c)*r1;
      const u32x4 o={pk(a[0],a[1]),pk(a[2],a[3]),pk(b[0],b[1]),pk(b[2],b[3])};
      *(LAS u32x4*)at(aS[f],0)=o; }
  }
  GAP();
  TS(4); TSRM(16);
  f32x4 hac[2][6], csh[3], c4[6], c5[6];
  #pragma unroll
  for(int k=0;k<3;k++) csh[k]=(f32x4){0.f,0.f,0.f,0.f};
  #pragma unroll
  for(int k=0;k<6;k++) hac[0][k]=hac[1][k]=(f32x4){0.f,0.f,0.f,0.f};
  float hm[2], hr[2];
  u64 pv=0; const unsigned pAB=WS_ABF+((((j>>3)*4+(j&3))*16+((j>>2)&1)*8)*2+(lane&15))*8;
  sfor<10>([&](auto T){ constexpr int t=T.value;
    #pragma unroll
    for(int s=0;s<2;s++){
      const bool last=(s==1);
      if(last && t==9){
        #pragma unroll
        for(int f=0;f<3;f++) *(LAS f32x4*)at(aH[0]+wc*384,128*f)=csh[f];
        hook(); GAP(); }
      wait_lgkm();
      if constexpr(CABM && t==9){ if(last && w==1){ wait_vm<0>(); poll_check(pv,p.ws,pAB,tag); TSR(17); } }
      if(last){ bar(); TBM(10+t); }
      if constexpr(t==HKEEP){ if(!last){
        #pragma unroll
        for(int k=0;k<4*(HKEEP-5);k++) asm volatile(""::"v"(xr[k>>2][(k>>1)&1][k&1])); } }
      GAP();
      #pragma unroll
      for(int m=0;m<15;m++){
        const bf16x8 (&xa)[2]= t<5? fa[s]: xr[t<5? 0: t-5][s];
        if(!last){ if(m<3) csh[m]=mma(gb[s][2*m],ones,csh[m]); else { const int mm=m-3, fm=mm/6, f0=mm%6, fn=fm?5-f0:f0; hac[fm][fn]=mma(gb[s][fn],xa[fm],hac[fm][fn]); } }
        else if(m<12){ const int fm=m/6, f0=m%6, fn=fm?5-f0:f0; hac[fm][fn]=mma(gb[s][fn],xa[fm],hac[fm][fn]); }
        if(!(last && t==9)) rdHs(m,(last? 12: 15)-HRE,last? t+1: t,s^1);
        else if(m<6){ c4[m]=*(const LAS f32x4*)at(aH[m&1],128*(m>>1)); c5[m]=*(const LAS f32x4*)at(aH[m&1],384+128*(m>>1)); }
        else if(m<8){ hm[m-6]=*(const LAS float*)at(hs[m-6],0); hr[m-6]=*(const LAS float*)at(hs[m-6],512); }
        if(t==T_HA && !last && m==3) h_addr();
        if constexpr(CABM && t==T_PA){ if(!last && m==12 && w==1) poll_issue(pv,p.ws,pAB); }
        GAP();
      }
    }
  });
  TS(5);
  const srd_t sH=mksrd(p.ws+WS_H+(size_t)(pm*48+3*j)*8192);
  #pragma unroll
  for(int f=0;f<6;f++) c4[f]+=c5[f];
  #pragma unroll
  for(int P=0;P<3;P++)
    #pragma unroll
    for(int fm=0;fm<2;fm++){ constexpr float L=1.4426950408889634f; const float yr=hr[fm]*L; u32x4 o;
      #pragma unroll
      for(int f2=0;f2<2;f2++){ const int f=2*P+f2; const f32x4 y=(hac[fm][f]-c4[f]*hm[fm])*yr; f32x4 e;
        #pragma unroll
        for(int k=0;k<4;k++) e[k]=__builtin_amdgcn_exp2f(-y[k]);
        e=e*L+L;
        #pragma unroll
        for(int k=0;k<4;k++) e[k]=__builtin_amdgcn_rcpf(e[k]);
        const f32x4 h=y*e; o[2*f2]=pk(h[0],h[1]); o[2*f2+1]=pk(h[2],h[3]); }
      __builtin_amdgcn_raw_buffer_store_b128(o,sH,hG[P]+fm*256,0,16); }
  TS(12);
  bar();
  TBM(TB1);
  TS(6); if(w!=1){ TSRM(17); }
  return tag;
}

template<class Hook>
DI void y_loop(const unsigned (&adA)[2][2], const unsigned (&adB)[2][2], f32x4 (&acc)[4][5], Hook hook){
  #pragma unroll
  for(int i=0;i<4;i++)
    #pragma unroll
    for(int k=0;k<5;k++) acc[i][k]=(f32x4){0.f,0.f,0.f,0.f};
  bf16x8 fa[2][4], fb[2][5];
  auto rd=[&](int m,int t,int ks,int bf){
    const int aw=slotA(t)>=65536, ao=slotA(t)-65536*aw, bw=slotB(t)==PB[3], bo=slotB(t)-(bw? PB[3]: PB[0]);
    if(m==0) fa[bf][0]=ldf(at(adA[aw][ks],ao));
    else if(m<=5) fb[bf][m-1]=ldf(at(adB[bw][ks],bo+(m-1)*2048));
    else fa[bf][m-5]=ldf(at(adA[aw][ks],ao+(m-5)*2048)); };
  #pragma unroll
  for(int m=0;m<9;m++) rd(m,0,0,0);
  GAP();
  sfor<NY>([&](auto T){ constexpr int t=T.value;
    sfor<2>([&](auto Sx){ constexpr int s=Sx.value; constexpr bool last=(s==1);
      wait_lgkm();
      if(last){ hook(T,ic<-1>{}); bar(); hook(T,ic<-2>{}); }
      GAP();
      sfor<20>([&](auto M){ constexpr int m=M.value, fm=m/5, f0=m%5, fn=(fm&1)?4-f0:f0;
        acc[fm][fn]=mma(fb[s][fn],fa[s][fm],acc[fm][fn]);
        if(m<9 && !(last && t==NY-1)) rd(m,last? t+1: t,last? 0: 1,s^1);
        hook(T,ic<s*20+m>{});
        GAP();
      });
    });
  });
}
struct GFrag { bf16x8 a[2][2], b[2][3]; };
struct GAddr { unsigned a[2][2], b[2][3][2]; };
template<int PART> DI void gate_addr(GAddr& r, LAS char* lds, unsigned w, unsigned lane, unsigned hq){
  const unsigned wr=w>>1, wc=w&1, g=lane>>4, c16=lane&15, lb=(unsigned)(size_t)lds;
  if constexpr(PART==0){
    #pragma unroll
    for(int ks=0;ks<2;ks++)
      #pragma unroll
      for(int a=0;a<2;a++){ r.a[ks][a]=lb+(4*ks+g)*1024+(32*wr+16*(a^wc)+c16)*16; PIN(r.a[ks][a]); } }
  else { constexpr int f=PART-1; const unsigned sw=(g&1)^hq, b0=lb+PB[0]+160*(8*g+(c16>>2))+8*(lane&3)+32*(wc? 4-f: f); r.b[0][f][0]=b0+sw*640; r.b[0][f][1]=b0+640-sw*640;
    #pragma unroll
    for(int k=0;k<2;k++){ r.b[1][f][k]=r.b[0][f][k]+(PB[3]-PB[0]); PIN(r.b[0][f][k]); PIN(r.b[1][f][k]); } } }
DI void gate_rd(int m, const GAddr& ad, int t, int h, GFrag& fr, s16x4 (&tl)[3]){
  const int ks=m/8, i=m%8, oA=slotA(t)+h*8192, bw=slotB(t)==PB[3], oB=slotB(t)-(bw? PB[3]: PB[0])+(h*64+ks*32)*160;
  if(i==0) fr.a[ks][0]=ldf(at(ad.a[ks][0],oA));
  else if(i<=6){ const int f=(i-1)/2; if(i&1) tl[f]=ldtr1(at(ad.b[bw][f][0],oB)); else fr.b[ks][f]=cat(tl[f],ldtr1(at(ad.b[bw][f][1],oB))); }
  else fr.a[ks][1]=ldf(at(ad.a[ks][1],oA)); }
template<class Hook>
DI void gate_loop(LAS char* lds, const GAddr& ad, GFrag (&fr)[2], f32x4 (&acc)[5], Hook hook){
  s16x4 tl[3];
  #pragma unroll
  for(int k=0;k<5;k++) acc[k]=(f32x4){0.f,0.f,0.f,0.f};
  sfor<NO>([&](auto T){ constexpr int t=NY+T.value;
    sfor<2>([&](auto Sx){ constexpr int s=Sx.value; constexpr bool last=(s==1);
      wait_lgkm();
      if(last){ hook(T,ic<-1>{}); if constexpr(TP2+t<=TLAST) bar(); hook(T,ic<-2>{}); }
      if constexpr(T.value>=NO-3){
        #pragma unroll
        for(int k=0;k<5;k++) PIN(acc[k]); }
      GAP();
      sfor<10>([&](auto M){ constexpr int m=M.value, ks=m/5, i=m%5, a=(i==2||i==3)? 1: 0, f= i==4? 2: (i&1);
        acc[i]=mma(fr[s].b[ks][f],fr[s].a[ks][a],acc[i]);
        if constexpr(!(last && t==NT-1)){
          if constexpr(m<6){ gate_rd(2*m,ad,last? t+1: t,last? 0: 1,fr[s^1],tl); gate_rd(2*m+1,ad,last? t+1: t,last? 0: 1,fr[s^1],tl); }
          else gate_rd(m+6,ad,last? t+1: t,last? 0: 1,fr[s^1],tl); }
        hook(T,ic<s*10+m>{});
        GAP();
      });
    });
  });
}
struct Qd { u32x4 a,b; u32x2 c; };
DI u32x2 pk4(const f32x4& v){ return (u32x2){pk(v[0],v[1]),pk(v[2],v[3])}; }
template<int OG,int OL> DI void send16(u64 mg,u64 ml,const u32x4& d,unsigned vg,const srd_t& srd,unsigned so,unsigned vl){
  asm volatile("s_mov_b64 exec, %0\n\tbuffer_store_dwordx4 %2, %3, %4, %5 offen offset:%7 sc1\n\ts_mov_b64 exec, %1\n\tds_write_b128 %6, %2 offset:%8\n\ts_mov_b64 exec, -1"
    ::"s"(mg),"s"(ml),"v"(d),"v"(vg),"s"(srd),"s"(so),"v"(vl),"n"(OG),"n"(OL):"memory"); }
#define OUTP 0
#define OUTAUX 0
#define OUTS ""
#define YS0 0
#define YSN 5
#define TADD 9
#define FPH 3
#define YBN 5
#define SP0 1
#define SPM 0
#define SP19 11
#define PLAG 3
#define T_YF 3
#define T_PY 7
#define T_CY 9

struct P2A { GAddr ad; unsigned xa[5], oL[3], oG[3], aY, aC, gY, gC, adA[2][2], adB[2][2]; bf16x8 sel[3]; };
template<int n> DI void p2_setup(P2A& A, LAS char* lds, int q, int pm, int pn, unsigned w, unsigned lane){
  const unsigned wr=w>>1, wc=w&1, g=lane>>4, c16=lane&15, lb=(unsigned)(size_t)lds;
  if constexpr(n<4) gate_addr<n>(A.ad,lds,w,lane,q>>1);
  if constexpr(n==4){
    const unsigned x0=lb+XOB+(32*wr+16*wc+c16)*160+8*g, x1=x0+(16-32*(int)wc)*160, xc0=wc? 128: 0, xc1=wc? 96: 32;
    A.xa[0]=x0+xc0; A.xa[1]=x0+xc1; A.xa[2]=x1+xc0; A.xa[3]=x1+xc1; A.xa[4]=x0+64;
    #pragma unroll
    for(int k=0;k<5;k++) PIN(A.xa[k]); }
  if constexpr(n>=5 && n<8){ constexpr int k=n-5; const unsigned iC=64+(lane&31);
    const unsigned r= k==0? 32*wr+16*(1-wc)+(lane>>2): k==1? 32*wr+16*wc+lane/6: 32*wr+16*wc+iC/6;
    const unsigned c= k==0? (wc? 96: 0)+(lane&3)*16: k==1? (wc? 64: 0)+(lane%6)*16: (wc? 64: 0)+(iC%6)*16;
    A.oL[k]=lb+XOB+r*160+c; A.oG[k]=r*(D_*2)+c; PIN(A.oL[k]); PIN(A.oG[k]); }
  if constexpr(n==8){ const unsigned rbase=((pm*4+pn)*4+q)*4*10240;
    A.aY=lb+YOWN+w*2048+lane*16; A.aC=lb+YOWN+8192+(w>>1)*1024+lane*16+(w&1)*8; A.gY=rbase+w*2048+lane*16; A.gC=rbase+8192+(w>>1)*1024+lane*16+(w&1)*8;
    PIN(A.aY); PIN(A.aC); PIN(A.gY); PIN(A.gC); }
  if constexpr(n==9){
    const unsigned one= g==(c16>>2)? 0x3F80u<<(16*(c16&1)): 0u, d0= (c16&2)? 0u: one, d1= (c16&2)? one: 0u;
    u32x4 s0={d0,d1,0u,0u}, s1={0u,0u,d0,d1}, s2={d0,d1,d0,d1};
    PIN(s0); PIN(s1); PIN(s2);
    A.sel[0]=__builtin_bit_cast(bf16x8,s0); A.sel[1]=__builtin_bit_cast(bf16x8,s1); A.sel[2]=__builtin_bit_cast(bf16x8,s2); }
  if constexpr(n==10){
    #pragma unroll
    for(int s=0;s<2;s++){ const unsigned ch=((4*s+g)^((lane>>1)&7))*16; A.adA[0][s]=lb+(wr*64+c16)*128+ch; A.adA[1][s]=A.adA[0][s]+65536;
      A.adB[0][s]=lb+PB[0]+(wc*80+c16)*128+ch; A.adB[1][s]=A.adB[0][s]+(PB[3]-PB[0]);
      PIN(A.adA[0][s]); PIN(A.adA[1][s]); PIN(A.adB[0][s]); PIN(A.adB[1][s]); } } }
DI void p2_mfma(const Prm& p, P2A& A, LAS char* lds, int q, int pm, int pn, unsigned w, unsigned lane, u64 tag, u16* out TSP){
  const int tile=pm*4+pn;
  const srd_t sY=mksrd(p.ws+WS_YX);
  const unsigned wr=w>>1, wc=w&1, g=lane>>4, c16=lane&15;
  const unsigned sbase=((tile*4+w)*4+q)*10240;
  const unsigned pY=WS_YF+((tile*4+q)*4+(lane&3))*8;
  u64 pv=0;
  Qd yq[4]; u32x4 yc[2];
  GFrag fr[2];
  const GAddr& ad=A.ad; const unsigned (&xa)[5]=A.xa; const unsigned (&oL)[3]=A.oL; const unsigned (&oG)[3]=A.oG; const bf16x8 (&sel)[3]=A.sel;
  const unsigned lb=(unsigned)(size_t)lds;
  u64 mG, mL; unsigned sb[3];
  asm volatile("s_cmp_eq_u32 %2, %3\n\ts_cselect_b64 %0, 0, -1\n\ts_cselect_b64 %1, -1, 0":"=&s"(mG),"=&s"(mL):"s"(w),"s"(q):"scc");
  #pragma unroll
  for(int k=0;k<3;k++){ sb[k]=rfl(sbase+k*4096); asm volatile("":"+s"(sb[k])); }
  { f32x4 acc[4][5]; u32x2 yp[4][5]; s16x4 tl[3];
    const unsigned pH= lane<32? WS_HF+(pm*16+(lane&15))*8: W2H? WS_HF+(((lane&31)>>1)*16+2*(2*pn+(q&1))+(lane&1))*8: WS_ABF+((2*pn+(q&1))*32+(lane&31))*8;
    auto pack1=[&](auto M){ constexpr int m=M.value, fm=m/5, f0=m%5, fn=(fm&1)?4-f0:f0; yp[fm][fn]=pk4(acc[fm][fn]); PIN(yp[fm][fn]); };
    if constexpr(SP19<1) p2_setup<10>(A,lds,q,pm,pn,w,lane);
    y_loop(A.adA,A.adB,acc,[&](auto T,auto M){ constexpr int t=T.value, m=M.value;
      if constexpr(m==-2){ TBM(TP2+t); }
      if constexpr(SPM==0){ if constexpr(t==SP0 && m==0) sfor<10>([&](auto N){ if constexpr(10-N.value>=SP19 ) p2_setup<N.value>(A,lds,q,pm,pn,w,lane); }); }
      else if constexpr(m==SPM && t>=SP0 && t-SP0<10){ if constexpr(10-(t-SP0)>=SP19) p2_setup<t-SP0>(A,lds,q,pm,pn,w,lane); }
      if constexpr(t==ST_FH && m==-1) wait_vm<0>();
      if constexpr(t==T_PH && m==17){ if(w==1) poll_issue(pv,p.ws,pH); }
      if constexpr(t==NY-4 && m==-1){ if(w==1){ wait_vm<0>(); poll_check(pv,p.ws,pH,tag); } }
      if constexpr(t==NY-1 && m>=20){
        if constexpr(m-20<16) gate_rd(m-20,ad,NY,0,fr[0],tl);
        if constexpr(m-20>=PLAG) pack1(ic<m-20-PLAG>{}); } });
    TS(7); TSR(18);
    sfor<PLAG>([&](auto M){ pack1(ic<20-PLAG+M.value>{}); });
    sfor<4>([&](auto K){ constexpr int W=K.value, wr_=W>>1, wc_=W&1, r0=2*wr_+wc_, r1=2*wr_+1-wc_, c0=wc_? 4: 0, c1=wc_? 3: 1;
      yq[W].a=(u32x4){yp[r0][c0][0],yp[r0][c0][1],yp[r0][c1][0],yp[r0][c1][1]};
      yq[W].b=(u32x4){yp[r1][c0][0],yp[r1][c0][1],yp[r1][c1][0],yp[r1][c1][1]}; });
    yc[0]=(u32x4){yp[0][2][0],yp[0][2][1],yp[1][2][0],yp[1][2][1]}; yc[1]=(u32x4){yp[2][2][0],yp[2][2][1],yp[3][2][0],yp[3][2][1]}; }
  GAP();
  unsigned y16=lane*16, l16=lb+YOWN+lane*16;
  asm volatile("":"+v"(y16),"+v"(l16));
  auto ysend=[&](auto N){ constexpr int n=N.value, h=n/5, i=n%5;
    if constexpr(i<4) send16<i*1024,h*4096+i*1024>(mG,mL,(i&1)? yq[2*h+i/2].b: yq[2*h+i/2].a,y16,sY,sb[h],l16);
    else send16<h*1024,8192+h*1024>(mG,mL,yc[h],y16,sY,sb[2],l16); };
  #pragma unroll
  for(int k=0;k<4;k++) asm volatile("":"+v"(yq[k].a),"+v"(yq[k].b));
  asm volatile("":"+v"(yc[0]),"+v"(yc[1]));
  sfor<YBN>([&](auto N){ ysend(N); });
  GAP();
  TS(8);
  f32x4 og[5], ty[5], xf[5]; Qd y[3], y0; u32x2 xv[5];
  auto yadd=[&](auto N){ constexpr int n=N.value, h=n/9, i=n%9;
    const f32x4 z={0.f,0.f,0.f,0.f};
    if constexpr(i<8){ constexpr int sn=2*h+i/4, k=i%4; const Qd& r= sn==0? y0: y[sn>0? sn-1: 0];
      ty[k]=mma(sel[k&1],__builtin_bit_cast(bf16x8,k<2? r.a: r.b),sn==0? z: ty[k]); }
    else { const u32x2 u= h==0? y0.c: y[1].c, v= h==0? y[0].c: y[2].c; const u32x4 uv={u[0],u[1],v[0],v[1]};
      ty[4]=mma(sel[2],__builtin_bit_cast(bf16x8,uv),h==0? z: ty[4]); }
    PIN(ty[i<8? i%4: 4]); };
  auto fetch=[&](auto N){ constexpr int n=N.value;
    constexpr int k=n/3, pt=n%3; const unsigned b=((q+1+k)&3)*10240;
    if constexpr(pt==0) y[k].a=__builtin_amdgcn_raw_buffer_load_b128(sY,A.gY,b,16);
    else if constexpr(pt==1) y[k].b=__builtin_amdgcn_raw_buffer_load_b128(sY,A.gY+1024,b,16);
    else y[k].c=__builtin_amdgcn_raw_buffer_load_b64(sY,A.gC,b,16); };
  const srd_t sO=mksrd(out+(size_t)(pm*128+(q>>1)*64)*D_+pn*160+(q&1)*80);
  gate_loop(lds,ad,fr,og,[&](auto T,auto M){ constexpr int t=T.value, m=M.value;
    if constexpr(m==-2 && t<NO-1){ TBM(TP2+NY+t); }
    if constexpr(m>=0){ constexpr int n=YBN+(2*t+m/10-YS0)*YSN+(m%10)/2;
      if constexpr(2*t+m/10>=YS0 && (m&1)==1 && (m%10)/2<YSN && n<10) ysend(ic<n>{}); }
    if constexpr(t==T_YF && m==-1){ wait_vm<0>(); if(lane==0) st_flag(p.ws,WS_YF+((tile*4+w)*4+q)*8,tag); }
    if constexpr(t==T_PY && m==8) poll_issue(pv,p.ws,pY);
    if constexpr(t==NO-2 && m>=11 && m<14){
      if constexpr(m==11) y0.a=*(const LAS u32x4*)at(A.aY,0); else if constexpr(m==12) y0.b=*(const LAS u32x4*)at(A.aY,1024); else y0.c=*(const LAS u32x2*)at(A.aC,0); }
    if constexpr(t==NO-1 && m>=0 && m<5) xv[m]=*(const LAS u32x2*)at(xa[m],0);
    if constexpr(t==NO-1 && m>=1 && m<1+TADD) yadd(ic<m-1>{});
    if constexpr(t==NO-1 && m>=10){ constexpr int k=(m-10)/2, e=(m-10)%2; xf[k][2*e]=bflo(xv[k][e]); xf[k][2*e+1]=bfhi(xv[k][e]); if constexpr(e==1) PIN(xf[k]); }
    if constexpr(t==T_CY && m==-1){ wait_vm<0>(); poll_check(pv,p.ws,pY,tag); }
    if constexpr(m>=0){ constexpr int hh=2*(t-T_CY)+m/10-1, n=hh*FPH+(m%10)/2;
      if constexpr(hh>=0 && (m&1)==1 && (m%10)/2<FPH && n<9) fetch(ic<n>{}); } });
  TS(9); TSR(19);
  { f32x4 e[5];
    sfor<5>([&](auto K){ constexpr int k=K.value, n0=TADD+k*((18-TADD+4)/5), nn=(18-TADD+4)/5;
      const f32x4 z=og[k]*-1.4426950408889634f;
      if constexpr(nn>0 && n0<18) yadd(ic<n0>{});
      e[k][0]=__builtin_amdgcn_exp2f(z[0]); e[k][1]=__builtin_amdgcn_exp2f(z[1]);
      GAP();
      if constexpr(nn>1 && n0+1<18) yadd(ic<n0+1>{});
      e[k][2]=__builtin_amdgcn_exp2f(z[2]); e[k][3]=__builtin_amdgcn_exp2f(z[3]);
      sfor<(nn>2? nn-2: 0)>([&](auto J){ if constexpr(n0+2+J.value<18) yadd(ic<n0+2+J.value>{}); });
      PIN(e[k]);
      GAP(); });
    TS(10);
    auto fin=[&](int k){ f32x4 r=e[k]+1.f;
      #pragma unroll
      for(int i=0;i<4;i++) r[i]=__builtin_amdgcn_rcpf(r[i]);
      const f32x4 o=(og[k]*ty[k])*r+xf[k];
      *(LAS u32x2*)at(xa[k],0)=pk4(o); };
    fin(2); fin(3);
    asm volatile("":::"memory");
    const u32x4 vA=*(const LAS u32x4*)at(oL[0],0);
    fin(0); fin(1);
    GAP();
    __builtin_amdgcn_raw_buffer_store_b128(vA,sO,oG[0],0,OUTAUX);
    fin(4);
    asm volatile("":::"memory");
    u32x4 vB=*(const LAS u32x4*)at(oL[1],0), vC=*(const LAS u32x4*)at(oL[2],0);
    PIN(vB); PIN(vC);
    asm volatile("buffer_store_dwordx4 %0, %2, %4, 0 offen" OUTS "\n\ts_mov_b32 exec_hi, 0\n\tbuffer_store_dwordx4 %1, %3, %4, 0 offen" OUTS "\n\ts_mov_b32 exec_hi, -1"
                 ::"v"(vB),"v"(vC),"v"(oG[1]),"v"(oG[2]),"s"(sO):"memory");
  }
  TS(11);
  if constexpr(CPF>0) asm volatile(".Lfnet_end:");
}
__global__ __launch_bounds__(512,2) void fnet_mlp_fused(Prm p){
  extern __shared__ __attribute__((aligned(1024))) char smem[];
  LAS char* lds=(LAS char*)smem;
  const unsigned tid=threadIdx.x, lane=tid&63, w=rfl(tid>>6);
  const int b=blockIdx.x, x8=b&7, r=b>>3;
  const int q=x8>>1, pm=(x8&1)*8+(r>>2), pn=r&3, j=q*4+pn;
  TSR(14); TS(0);
  u32x4 sv[5];
  #pragma unroll
  for(int i=0;i<5;i++) asm volatile("":"=v"(sv[i]));
  if(__builtin_expect(w<2,1)){ const unsigned row=pm*128+j*8+w*4+(lane>>4);
    const u16* a=p.x+(size_t)row*D_+(lane&15)*8;
    asm volatile("global_load_dwordx4 %0, %5, off\n\tglobal_load_dwordx4 %1, %5, off offset:256\n\tglobal_load_dwordx4 %2, %5, off offset:512\n\t"
                 "global_load_dwordx4 %3, %5, off offset:768\n\tglobal_load_dwordx4 %4, %5, off offset:1024"
                 :"+v"(sv[0]),"+v"(sv[1]),"+v"(sv[2]),"+v"(sv[3]),"+v"(sv[4]):"v"(a):"memory"); }
  unsigned cpf; asm volatile("":"=v"(cpf));
  if constexpr(CPF>0){ if(__builtin_expect(w==0,1)){ u64 pc; unsigned len;
      asm volatile("s_getpc_b64 %0\n.Lfnet_pc:\n\ts_mov_b32 %1, .Lfnet_end-.Lfnet_pc":"=s"(pc),"=s"(len));
      const unsigned o=(r*CPF+(lane<CPF? lane: 0))*128;
      const char* a=(const char*)pc+(o<len? o: 0);
      asm volatile("global_load_dword %0, %1, off":"+v"(cpf):"v"(a):"memory"); } }
  if(__builtin_expect(w>=4,1)){
    const unsigned lw=w-4;
    const u64 tag=loader(p,lds,pm,j,q,pn,lw,lane TSA);
    TS(8);
    if(b==0 && lw==0){
      #pragma unroll 1
      for(int i=0;i<4;i++) wait_flags(p.ws,WS_ABF+(i*64+lane)*8,tag);
      if(lane==0) st_flag(p.ws,0,tag);
    }
  } else {
    u16* out;
    P2A A;
    const u64 tag=p1_mfma(p,lds,pm,j,w,lane,cpf,sv,out,[&](){ sfor<11>([&](auto N){ if constexpr(10-N.value<SP19) p2_setup<N.value>(A,lds,q,pm,pn,w,lane); }); } TSA);
    p2_mfma(p,A,lds,q,pm,pn,w,lane,tag,out TSA);
  }
  TSR(15); TSPUT();
}

extern "C" void kernel_launch(void* const* d_in, const int* in_sizes, int n_in, void* d_out, int out_size, void* d_ws, size_t ws_size, hipStream_t stream) {
  Prm p; p.x=(const u16*)d_in[0]; p.wsre=(const u16*)d_in[1]; p.wsim=(const u16*)d_in[2]; p.wdre=(const u16*)d_in[3]; p.wdim=(const u16*)d_in[4];
  p.w1=(const u16*)d_in[6]; p.w2=(const u16*)d_in[7]; p.out=(u16*)d_out; p.ws=(char*)d_ws+WSOFF;
  (void)hipFuncSetAttribute((const void*)fnet_mlp_fused, hipFuncAttributeMaxDynamicSharedMemorySize, LDS_BYTES);
  fnet_mlp_fused<<<256,512,LDS_BYTES,stream>>>(p);
}
```
